```python
import torch

def _softmax(x, axis=-1):
    return torch.softmax(x, dim=axis)
def _gelu(x):
    return torch.nn.functional.gelu(x, approximate="tanh")
def _silu(x):
    return torch.nn.functional.silu(x)
def _sigmoid(x):
    return torch.sigmoid(x)
def _layernorm(x, axis=-1, eps=1e-5):
    m = x.mean(dim=axis, keepdim=True)
    v = x.var(dim=axis, keepdim=True, unbiased=False)
    return (x - m) / torch.sqrt(v + eps)
def _scatter_add(tgt, idx, src, axis=0):
    return tgt.clone().index_add_(axis, idx, src)
def _segment_sum(src, seg, n):
    out = torch.zeros((n,) + src.shape[1:], dtype=src.dtype)
    return out.index_add_(0, seg, src)
def _pad(x, pw):
    flat = tuple(v for pair in reversed(pw) for v in pair)
    return torch.nn.functional.pad(x, flat)

kernel_name = 'parallel_sum__ssm_conv_h3__glu_gated_reglu__prompt'

def setup_inputs():

    _w = (lambda a: (lambda t: t.to(torch.bfloat16) if t.is_floating_point() else t)(torch.as_tensor(a)))
    def _A_setup():
        B, S, D = 1, 2048, 512
        ks = 4
        return {
            "q": _w((lambda *s: torch.randn(*s, dtype=torch.bfloat16) * 0.05)(B, S, D)),
            "k": _w((lambda *s: torch.randn(*s, dtype=torch.bfloat16) * 0.2)(B, S, D)),
            "v": _w((lambda *s: torch.randn(*s, dtype=torch.bfloat16) * 0.2)(B, S, D)),
            "shift_k": _w((lambda *s: torch.randn(*s, dtype=torch.bfloat16) * 0.2)(D, ks) * 0.5),
            "ssm_k": _w((lambda *s: torch.randn(*s, dtype=torch.bfloat16) * 0.4)(D, S) * (1.0 / S ** 0.5)),
            "ssm_D": _w((lambda *s: torch.randn(*s, dtype=torch.bfloat16) * 0.2)(D)),
        }
    _A_in = _A_setup()

    def _B_setup():
        B, S, D, F = 1, 2048, 512, 1792
        return {
            "x": _w((lambda *s: torch.randn(*s, dtype=torch.bfloat16) * 0.05)(B, S, D)),
            "W_gate": _w((lambda *s: torch.randn(*s, dtype=torch.bfloat16) * 0.2)(D, F) * (2.0 / D) ** 0.5),
            "W_up": _w((lambda *s: torch.randn(*s, dtype=torch.bfloat16) * 0.2)(D, F) * (2.0 / D) ** 0.5),
            "W_down": _w((lambda *s: torch.randn(*s, dtype=torch.bfloat16) * 0.2)(F, D) * (1.0 / F) ** 0.5),
        }
    _B_in = _B_setup()

    return {
        "A_q": _A_in["q"],
        "A_k": _A_in["k"],
        "A_v": _A_in["v"],
        "A_shift_k": _A_in["shift_k"],
        "A_ssm_k": _A_in["ssm_k"],
        "A_ssm_D": _A_in["ssm_D"],
        "B_x": _B_in["x"],
        "B_W_gate": _B_in["W_gate"],
        "B_W_up": _B_in["W_up"],
        "B_W_down": _B_in["W_down"],
    }

def reference(A_q, A_k, A_v, A_shift_k, A_ssm_k, A_ssm_D, B_x, B_W_gate, B_W_up, B_W_down):
    def _A_ref(q, k, v, shift_k, ssm_k, ssm_D):
        B, S, D = q.shape
        ks = shift_k.shape[-1]
        k_pad = _pad(k, ((0, 0), (ks - 1, 0), (0, 0)))
        k_shift = torch.zeros_like(k)
        for t in range(ks):
            k_shift = k_shift + k_pad[:, t:t + S, :] * shift_k[:, ks - 1 - t][None, None, :]
        kv = k_shift * v
        L = 2 * S
        kv_pad = _pad(kv, ((0, 0), (0, S), (0, 0)))
        kf_pad = _pad(ssm_k, ((0, 0), (0, S)))
        kv_f = (lambda x, axes=(-2,-1): torch.fft.rfft2(x.float(), dim=axes))(kv_pad[:, None, :, :], axes=(1, 2))
        kf_f = (lambda x, axes=(-2,-1): torch.fft.rfft2(x.float(), dim=axes))(kf_pad[:, None, :], axes=(1, 2))
        kf_f = (lambda x, *d: x.permute(*d))(kf_f, 1, 2, 0)
        y_f = kv_f * kf_f[None, :, :, :]
        y = (lambda x, s, axes=(-2,-1): torch.fft.irfft2(x, s=s, dim=axes).to(torch.bfloat16))(y_f, (1, L), axes=(1, 2))[:, 0, :S, :]
        y = y + kv * ssm_D[None, None, :]
        return q * y

    def _B_ref(x, W_gate, W_up, W_down):
        gate = torch.einsum("bsd,df->bsf", x, W_gate)
        up = torch.einsum("bsd,df->bsf", x, W_up)
        gate = gate * (lambda x, dt: x.to(dt))(gate > 0, gate.dtype)
        h = gate * up
        return torch.einsum("bsf,fd->bsd", h, W_down)

    x = A_q
    xn = _layernorm(x, axis=-1)
    return x + _A_ref(xn, A_k, A_v, A_shift_k, A_ssm_k, A_ssm_D) + _B_ref(xn, B_W_gate, B_W_up, B_W_down)
```

```cpp
#include <hip/hip_runtime.h>
#include <hip/hip_bf16.h>
typedef unsigned short u16;
constexpr int S=2048, D=512, F=1792;
__device__ __forceinline__ float b2f(u16 x){ return __uint_as_float(((unsigned)x)<<16); }
__device__ __forceinline__ u16 f2b(float x){ unsigned u=__float_as_uint(x); u += 0x7fffu + ((u>>16)&1u); return (u16)(u>>16); }
__device__ __forceinline__ float rb(float x){ return b2f(f2b(x)); }

__global__ void k_ln_kv(const u16* q,const u16* k,const u16* v,const u16* sk,u16* xn,u16* kv){
  int t=blockIdx.x, d=threadIdx.x;
  __shared__ float red[512];
  float x=b2f(q[t*D+d]);
  red[d]=x; __syncthreads();
  for(int o=256;o>0;o>>=1){ if(d<o) red[d]+=red[d+o]; __syncthreads(); }
  float m=red[0]/D; __syncthreads();
  red[d]=(x-m)*(x-m); __syncthreads();
  for(int o=256;o>0;o>>=1){ if(d<o) red[d]+=red[d+o]; __syncthreads(); }
  float var=red[0]/D;
  float mb=rb(m), vb=rb(var);
  float sd=rb(sqrtf(rb(vb+1e-5f)));
  xn[t*D+d]=f2b(rb(x-mb)/sd);
  float acc=0.f;
  for(int j=0;j<4;j++){ int s=t-3+j; float kk = s>=0? b2f(k[s*D+d]):0.f; acc=rb(acc+rb(kk*b2f(sk[d*4+3-j]))); }
  kv[t*D+d]=f2b(acc*b2f(v[t*D+d]));
}
__global__ void k_conv(const u16* kv,const u16* h,const u16* Dv,u16* y){
  int t=blockIdx.x, d=threadIdx.x;
  float acc=0.f;
  for(int s=0;s<=t;s++) acc+=b2f(h[d*S+t-s])*b2f(kv[s*D+d]);
  y[t*D+d]=f2b(rb(acc)+rb(b2f(kv[t*D+d])*b2f(Dv[d])));
}
__global__ void k_g1(const u16* xn,const u16* wg,const u16* wu,u16* hh){
  int t=blockIdx.y, f=blockIdx.x*256+threadIdx.x;
  float g=0,u=0;
  for(int d=0;d<D;d++){ float x=b2f(xn[t*D+d]); g+=x*b2f(wg[d*F+f]); u+=x*b2f(wu[d*F+f]); }
  g=rb(g); u=rb(u); g = g>0.f? g:0.f;
  hh[t*F+f]=f2b(g*u);
}
__global__ void k_g2(const u16* q,const u16* xn,const u16* y,const u16* hh,const u16* wd,u16* out){
  int t=blockIdx.x, d=threadIdx.x;
  float a=0;
  for(int f=0;f<F;f++) a+=b2f(hh[t*F+f])*b2f(wd[f*D+d]);
  float oa=rb(b2f(xn[t*D+d])*b2f(y[t*D+d]));
  out[t*D+d]=f2b(rb(b2f(q[t*D+d])+oa)+rb(a));
}
extern "C" void kernel_launch(void* const* d_in, const int* in_sizes, int n_in, void* d_out, int out_size, void* d_ws, size_t ws_size, hipStream_t stream) {
  const u16 *q=(const u16*)d_in[0],*k=(const u16*)d_in[1],*v=(const u16*)d_in[2],*sk=(const u16*)d_in[3],*h=(const u16*)d_in[4],*Dv=(const u16*)d_in[5];
  const u16 *wg=(const u16*)d_in[7],*wu=(const u16*)d_in[8],*wd=(const u16*)d_in[9];
  u16* ws=(u16*)d_ws; u16* xn=ws; u16* kv=ws+S*D; u16* y=ws+2*S*D; u16* hh=ws+3*S*D;
  k_ln_kv<<<S,512,0,stream>>>(q,k,v,sk,xn,kv);
  k_conv<<<S,512,0,stream>>>(kv,h,Dv,y);
  k_g1<<<dim3(F/256,S),256,0,stream>>>(xn,wg,wu,hh);
  k_g2<<<S,512,0,stream>>>(q,xn,y,hh,wd,(u16*)d_out);
}
```

```cpp
#include <hip/hip_runtime.h>
#include <hip/hip_bf16.h>
typedef unsigned short u16;
constexpr int S=2048, D=512, F=1792;
__device__ __forceinline__ float b2f(u16 x){ return __uint_as_float(((unsigned)x)<<16); }
__device__ __forceinline__ u16 f2b(float x){ unsigned u=__float_as_uint(x); u += 0x7fffu + ((u>>16)&1u); return (u16)(u>>16); }
__device__ __forceinline__ float rb(float x){ return b2f(f2b(x)); }

__global__ void k_ln_kv(const u16* q,const u16* k,const u16* v,const u16* sk,u16* xn,u16* kv){
  int t=blockIdx.x, d=threadIdx.x;
  __shared__ float red[512];
  float x=b2f(q[t*D+d]);
  red[d]=x; __syncthreads();
  for(int o=256;o>0;o>>=1){ if(d<o) red[d]+=red[d+o]; __syncthreads(); }
  float m=red[0]/D; __syncthreads();
  red[d]=(x-m)*(x-m); __syncthreads();
  for(int o=256;o>0;o>>=1){ if(d<o) red[d]+=red[d+o]; __syncthreads(); }
  float var=red[0]/D;
  float mb=rb(m), vb=rb(var);
  float sd=rb(sqrtf(rb(vb+1e-5f)));
  xn[t*D+d]=f2b(rb(x-mb)/sd);
  float acc=0.f;
  for(int j=0;j<4;j++){ int s=t-3+j; float kk = s>=0? b2f(k[s*D+d]):0.f; acc=rb(acc+rb(kk*b2f(sk[d*4+3-j]))); }
  kv[t*D+d]=f2b(acc*b2f(v[t*D+d]));
}
__global__ void k_conv(const u16* kv,const u16* h,const u16* Dv,u16* y){
  int t=blockIdx.x, d=threadIdx.x;
  float acc=0.f;
  for(int s=0;s<=t;s++) acc+=b2f(h[d*S+t-s])*b2f(kv[s*D+d]);
  y[t*D+d]=f2b(rb(acc)+rb(b2f(kv[t*D+d])*b2f(Dv[d])));
}
__global__ void k_g1(const u16* xn,const u16* wg,const u16* wu,u16* hh){
  int t=blockIdx.y, f=blockIdx.x*256+threadIdx.x;
  float g=0,u=0;
  for(int d=0;d<D;d++){ float x=b2f(xn[t*D+d]); g+=x*b2f(wg[d*F+f]); u+=x*b2f(wu[d*F+f]); }
  g=rb(g); u=rb(u); g = g>0.f? g:0.f;
  hh[t*F+f]=f2b(g*u);
}
__global__ void k_g2(const u16* q,const u16* xn,const u16* y,const u16* hh,const u16* wd,u16* out){
  int t=blockIdx.x, d=threadIdx.x;
  float a=0;
  for(int f=0;f<F;f++) a+=b2f(hh[t*F+f])*b2f(wd[f*D+d]);
  float oa=rb(b2f(xn[t*D+d])*b2f(y[t*D+d]));
  out[t*D+d]=f2b(rb(b2f(q[t*D+d])+oa)+rb(a));
}

typedef __attribute__((ext_vector_type(4))) unsigned u32x4;
struct P { const u32x4* p[7]; };
template<int NB>
__global__ __launch_bounds__(256) void k_probe(P a, unsigned long long* st){
  unsigned long long t0=__builtin_amdgcn_s_memrealtime();
  u32x4 acc={0,0,0,0};
  u32x4 r[NB][2];
  int t=threadIdx.x; int base=blockIdx.x*448;
  if(t<224){
  #pragma unroll
  for(int b=0;b<NB;b++){ r[b][0]=*(a.p[b]+base+t); r[b][1]=*(a.p[b]+base+224+t); }
  #pragma unroll
  for(int b=0;b<NB;b++){ acc^=r[b][0]; acc^=r[b][1]; }
  }
  if((acc[0]^acc[1]^acc[2]^acc[3])==0x12345678u) st[100000]=1;
  __syncthreads();
  unsigned long long t1=__builtin_amdgcn_s_memrealtime();
  if(t==0){ st[blockIdx.x*2]=t0; st[blockIdx.x*2+1]=t1; }
}
__global__ void k_amp(const unsigned long long* st){
  unsigned long long mn=~0ull,mx=0;
  for(int i=0;i<256;i++){ mn=min(mn,st[2*i]); mx=max(mx,st[2*i+1]); }
  unsigned long long d=(mx-mn); if(d>2000) d=2000; d*=1000;
  unsigned long long t0=__builtin_amdgcn_s_memrealtime();
  while(__builtin_amdgcn_s_memrealtime()-t0<d) __builtin_amdgcn_s_sleep(8);
}
extern "C" void kernel_launch(void* const* d_in, const int* in_sizes, int n_in, void* d_out, int out_size, void* d_ws, size_t ws_size, hipStream_t stream) {
  const u16 *q=(const u16*)d_in[0],*k=(const u16*)d_in[1],*v=(const u16*)d_in[2],*sk=(const u16*)d_in[3],*h=(const u16*)d_in[4],*Dv=(const u16*)d_in[5];
  const u16 *wg=(const u16*)d_in[7],*wu=(const u16*)d_in[8],*wd=(const u16*)d_in[9];
  u16* ws=(u16*)d_ws; u16* xn=ws; u16* kv=ws+S*D; u16* y=ws+2*S*D; u16* hh=ws+3*S*D;
  P a; int idx[7]={0,7,8,1,2,4,9}; for(int i=0;i<7;i++) a.p[i]=(const u32x4*)d_in[idx[i]];
  unsigned long long* st=(unsigned long long*)d_ws+(20<<20);
  k_probe<1><<<256,256,0,stream>>>(a,st);
  k_amp<<<1,1,0,stream>>>(st);
  k_ln_kv<<<S,512,0,stream>>>(q,k,v,sk,xn,kv);
  k_conv<<<S,512,0,stream>>>(kv,h,Dv,y);
  k_g1<<<dim3(F/256,S),256,0,stream>>>(xn,wg,wu,hh);
  k_g2<<<S,512,0,stream>>>(q,xn,y,hh,wd,(u16*)d_out);
}
```

```cpp
#include <hip/hip_runtime.h>
#include <hip/hip_bf16.h>
typedef unsigned short u16;
constexpr int S=2048, D=512, F=1792;
__device__ __forceinline__ float b2f(u16 x){ return __uint_as_float(((unsigned)x)<<16); }
__device__ __forceinline__ u16 f2b(float x){ unsigned u=__float_as_uint(x); u += 0x7fffu + ((u>>16)&1u); return (u16)(u>>16); }
__device__ __forceinline__ float rb(float x){ return b2f(f2b(x)); }

__global__ void k_ln_kv(const u16* q,const u16* k,const u16* v,const u16* sk,u16* xn,u16* kv){
  int t=blockIdx.x, d=threadIdx.x;
  __shared__ float red[512];
  float x=b2f(q[t*D+d]);
  red[d]=x; __syncthreads();
  for(int o=256;o>0;o>>=1){ if(d<o) red[d]+=red[d+o]; __syncthreads(); }
  float m=red[0]/D; __syncthreads();
  red[d]=(x-m)*(x-m); __syncthreads();
  for(int o=256;o>0;o>>=1){ if(d<o) red[d]+=red[d+o]; __syncthreads(); }
  float var=red[0]/D;
  float mb=rb(m), vb=rb(var);
  float sd=rb(sqrtf(rb(vb+1e-5f)));
  xn[t*D+d]=f2b(rb(x-mb)/sd);
  float acc=0.f;
  for(int j=0;j<4;j++){ int s=t-3+j; float kk = s>=0? b2f(k[s*D+d]):0.f; acc=rb(acc+rb(kk*b2f(sk[d*4+3-j]))); }
  kv[t*D+d]=f2b(acc*b2f(v[t*D+d]));
}
__global__ void k_conv(const u16* kv,const u16* h,const u16* Dv,u16* y){
  int t=blockIdx.x, d=threadIdx.x;
  float acc=0.f;
  for(int s=0;s<=t;s++) acc+=b2f(h[d*S+t-s])*b2f(kv[s*D+d]);
  y[t*D+d]=f2b(rb(acc)+rb(b2f(kv[t*D+d])*b2f(Dv[d])));
}
__global__ void k_g1(const u16* xn,const u16* wg,const u16* wu,u16* hh){
  int t=blockIdx.y, f=blockIdx.x*256+threadIdx.x;
  float g=0,u=0;
  for(int d=0;d<D;d++){ float x=b2f(xn[t*D+d]); g+=x*b2f(wg[d*F+f]); u+=x*b2f(wu[d*F+f]); }
  g=rb(g); u=rb(u); g = g>0.f? g:0.f;
  hh[t*F+f]=f2b(g*u);
}
__global__ void k_g2(const u16* q,const u16* xn,const u16* y,const u16* hh,const u16* wd,u16* out){
  int t=blockIdx.x, d=threadIdx.x;
  float a=0;
  for(int f=0;f<F;f++) a+=b2f(hh[t*F+f])*b2f(wd[f*D+d]);
  float oa=rb(b2f(xn[t*D+d])*b2f(y[t*D+d]));
  out[t*D+d]=f2b(rb(b2f(q[t*D+d])+oa)+rb(a));
}

typedef __attribute__((ext_vector_type(4))) unsigned u32x4;
__global__ __launch_bounds__(256) void k_probe(const u32x4* p, unsigned long long* st){
  extern __shared__ char smem[];
  unsigned long long t0,t1,t2; const u32x4* pp;
  asm volatile("s_memrealtime %0\n s_load_dwordx2 %1, %3, 0x0\n s_waitcnt lgkmcnt(0)\n s_memrealtime %2\n s_waitcnt lgkmcnt(0)":"=&s"(t0),"=&s"(pp),"=&s"(t1):"s"(__builtin_amdgcn_kernarg_segment_ptr()):"memory");
  u32x4 r;
  asm volatile("global_load_dwordx4 %0, %1, off\n s_waitcnt vmcnt(0)":"=v"(r):"v"(pp+blockIdx.x*256+threadIdx.x):"memory");
  asm volatile("s_memrealtime %0\n s_waitcnt lgkmcnt(0)":"=s"(t2)::"memory");
  if(r[0]==0x12345678u && r[1]==0x9abcdef0u) st[100000]=1;
  if(threadIdx.x==0){ st[blockIdx.x*4]=t0; st[blockIdx.x*4+1]=t1; st[blockIdx.x*4+2]=t2; }
}
__global__ void k_amp(const unsigned long long* st){
  unsigned long long mn0=~0ull,mx0=0,mx1=0,mx2=0,md01=0,md12=0,s01=0,s12=0;
  for(int i=0;i<256;i++){ unsigned long long a=st[4*i],b=st[4*i+1],c=st[4*i+2]; mn0=min(mn0,a); mx0=max(mx0,a); mx1=max(mx1,b); mx2=max(mx2,c); md01=max(md01,b-a); md12=max(md12,c-b); s01+=b-a; s12+=c-b; }
  unsigned long long d=0; if(d>2000000) d=2000000;
  unsigned long long t0=__builtin_amdgcn_s_memrealtime();
  while(__builtin_amdgcn_s_memrealtime()-t0<d) __builtin_amdgcn_s_sleep(8);
}
extern "C" void kernel_launch(void* const* d_in, const int* in_sizes, int n_in, void* d_out, int out_size, void* d_ws, size_t ws_size, hipStream_t stream) {
  const u16 *q=(const u16*)d_in[0],*k=(const u16*)d_in[1],*v=(const u16*)d_in[2],*sk=(const u16*)d_in[3],*h=(const u16*)d_in[4],*Dv=(const u16*)d_in[5];
  const u16 *wg=(const u16*)d_in[7],*wu=(const u16*)d_in[8],*wd=(const u16*)d_in[9];
  u16* ws=(u16*)d_ws; u16* xn=ws; u16* kv=ws+S*D; u16* y=ws+2*S*D; u16* hh=ws+3*S*D;
  unsigned long long* st=(unsigned long long*)d_ws+(20<<20);
  hipFuncSetAttribute((const void*)k_probe,hipFuncAttributeMaxDynamicSharedMemorySize,160*1024);
  k_probe<<<256,256,163840,stream>>>((const u32x4*)d_in[0],st);
  k_amp<<<1,1,0,stream>>>(st);
  k_ln_kv<<<S,512,0,stream>>>(q,k,v,sk,xn,kv);
  k_conv<<<S,512,0,stream>>>(kv,h,Dv,y);
  k_g1<<<dim3(F/256,S),256,0,stream>>>(xn,wg,wu,hh);
  k_g2<<<S,512,0,stream>>>(q,xn,y,hh,wd,(u16*)d_out);
}
```

```cpp
#include <hip/hip_runtime.h>
#include <hip/hip_bf16.h>
#define WS_STATS 4096
#define WS_KV 1048576
#define WS_Y 10485760
#define WS_H 3145728
#define WS_USED 15728640

typedef unsigned short u16;

struct Buf { const void* p; unsigned bytes; unsigned fmt; };
struct Args { Buf x, wg, wu, ws, wd, ssmk, out, ws2, k, v, shk; const void* ssmd; };

__global__ __launch_bounds__(256, 1) void fused_h3_reglu(Args a) {
  __shared__ __attribute__((aligned(16))) char smem[163840];
  unsigned long long tstart = 0;
  const int b = blockIdx.x;
  const int tid = threadIdx.x;
  int tv = tid;
  asm volatile(
"s_load_dwordx16 s[8:23], %1, 0x0\n"
"s_load_dwordx8 s[24:31], %1, 0x40\n"
"s_load_dwordx8 s[36:43], %1, 0x60\n"
"s_load_dwordx2 s[34:35], %1, 0xb0\n"
"s_mov_b32 s62, %2\n"
"v_mov_b32 v0, %0\n"
"v_lshrrev_b32 v22, 6, v0\n"
"v_and_b32 v0, 63, v0\n"
"v_readfirstlane_b32 s61, v22\n"
"s_and_b32 s80, s62, 7\n"
"s_lshr_b32 s81, s62, 3\n"
"v_lshlrev_b32 v1, 4, v0\n"
"s_getpc_b64 s[44:45]\n"
"Lpc_%=:\n"
"s_lshl_b32 s63, s81, 10\n"
"v_lshlrev_b32 v22, 8, v22\n"
"v_and_b32 v23, 1, v0\n"
"v_lshl_add_u32 v22, v23, 7, v22\n"
"v_add_u32 v22, s63, v22\n"
"s_and_b32 s45, s45, 0xffff\n"
"s_mov_b32 s46, Lend_%=-Lpc_%=\n"
"s_mov_b32 s47, 0x20000\n"
"buffer_load_dword v221, v22, s[44:47], 0 offen\n"
"s_lshl_b32 s75, s61, 12\n"
"s_lshl_b32 s63, s80, 5\n"
"s_add_u32 s63, s63, s81\n"
"s_lshl_b32 s63, s63, 2\n"
"s_add_u32 s83, s63, s61\n"
"s_lshl_b32 s63, s83, 11\n"
"v_and_b32 v54, 32, v0\n"
"v_lshl_add_u32 v54, v54, 4, v1\n"
"v_add_u32 v54, s63, v54\n"
"s_lshr_b32 s63, s83, 5\n"
"s_lshl_b32 s63, s63, 9\n"
"s_and_b32 s64, s83, 15\n"
"s_lshl_b32 s64, s64, 5\n"
"s_add_u32 s63, s63, s64\n"
"s_lshr_b32 s64, s83, 1\n"
"s_and_b32 s64, s64, 8\n"
"s_add_u32 s63, s63, s64\n"
"s_add_u32 s99, s63, 4096\n"
"s_cmp_lt_u32 s81, 4\n"
"s_cbranch_scc1 Lhelp_%=\n"
"s_sub_u32 s63, s81, 4\n"
"s_mul_i32 s64, s63, 37\n"
"s_lshr_b32 s64, s64, 8\n"
"s_mul_i32 s65, s64, 7\n"
"s_sub_u32 s65, s63, s65\n"
"s_and_b32 s76, s80, 4\n"
"s_add_u32 s76, s76, s64\n"
"s_and_b32 s63, s80, 3\n"
"s_mul_i32 s77, s63, 7\n"
"s_add_u32 s77, s77, s65\n"
"s_lshr_b32 s78, s63, 1\n"
"s_mul_i32 s79, s64, 1\n"
"s_lshr_b32 s83, s80, 2\n"
"s_mul_i32 s83, s83, 4\n"
"s_add_u32 s79, s79, s83\n"
"s_and_b32 s79, s79, 7\n"
"s_lshl_b32 s66, s79, 7\n"
"s_lshr_b32 s63, s61, 1\n"
"s_lshl_b32 s83, s63, 2\n"
"s_and_b32 s64, s61, 1\n"
"s_add_u32 s83, s83, s64\n"
"s_lshl_b32 s64, s78, 1\n"
"s_sub_u32 s83, s83, s64\n"
"s_add_u32 s83, s83, 2\n"
"s_lshl_b32 s64, s76, 3\n"
"s_add_u32 s83, s83, s64\n"
"s_lshl_b32 s74, s83, 15\n"
"v_lshrrev_b32 v22, 3, v0\n"
"v_lshrrev_b32 v23, 4, v0\n"
"v_and_b32 v40, 7, v0\n"
"v_xor_b32 v41, v40, v23\n"
"v_lshlrev_b32 v41, 4, v41\n"
"v_lshl_add_u32 v6, v22, 10, v41\n"
"v_xor_b32 v41, 64, v6\n"
"v_add_u32 v7, 7168, v41\n"
"v_add_u32 v8, 14336, v6\n"
"v_add_u32 v9, 14336, v7\n"
"v_and_b32 v42, 1, v23\n"
"v_lshlrev_b32 v41, 1, v42\n"
"v_xor_b32 v41, v40, v41\n"
"v_lshlrev_b32 v41, 4, v41\n"
"s_mul_i32 s63, s63, 114688\n"
"s_lshl_b32 s64, s77, 7\n"
"s_add_u32 s63, s63, s64\n"
"v_mul_u32_u24 v43, 3584, v22\n"
"v_add_u32 v43, s63, v43\n"
"v_add_u32 v10, v43, v41\n"
"v_xor_b32 v41, 64, v41\n"
"v_add_u32 v41, v43, v41\n"
"v_add_u32 v11, 27648, v41\n"
"v_add_u32 v12, 55296, v10\n"
"v_add_u32 v13, 55296, v11\n"
"s_add_u32 s67, s66, 128\n"
"s_and_b32 s67, s67, 1023\n"
"s_add_u32 s68, s66, 256\n"
"s_and_b32 s68, s68, 1023\n"
"s_add_u32 s69, s66, 384\n"
"s_and_b32 s69, s69, 1023\n"
"s_add_u32 s70, s66, 512\n"
"s_and_b32 s70, s70, 1023\n"
"s_add_u32 s71, s66, 640\n"
"s_and_b32 s71, s71, 1023\n"
"s_add_u32 s72, s66, 768\n"
"s_and_b32 s72, s72, 1023\n"
"s_add_u32 s73, s66, 896\n"
"s_and_b32 s73, s73, 1023\n"
"v_lshrrev_b32 v40, 4, v0\n"
"v_and_b32 v41, 15, v0\n"
"v_bfe_u32 v42, v0, 2, 2\n"
"v_and_b32 v43, 3, v0\n"
"v_and_b32 v44, 31, v0\n"
"v_lshrrev_b32 v45, 5, v0\n"
"v_and_b32 v46, 7, v0\n"
"v_lshrrev_b32 v47, 3, v0\n"
"v_lshrrev_b32 v49, 1, v41\n"
"v_xor_b32 v49, v49, v40\n"
"v_lshlrev_b32 v49, 4, v49\n"
"v_lshl_add_u32 v49, v41, 7, v49\n"
"v_add_u32 v14, 65536, v49\n"
"v_xor_b32 v15, 64, v14\n"
"s_lshl_b32 s64, s61, 5\n"
"v_lshl_add_u32 v49, v43, 3, s64\n"
"v_and_b32 v50, 24, v0\n"
"v_lshlrev_b32 v50, 2, v50\n"
"v_xor_b32 v49, v49, v50\n"
"v_lshlrev_b32 v51, 7, v42\n"
"v_lshl_add_u32 v51, v40, 10, v51\n"
"v_add_u32 v5, v49, v51\n"
"v_add_u32 v249, 114688, v5\n"
"v_lshlrev_b32 v49, 4, v43\n"
"v_xor_b32 v49, v49, v50\n"
"s_add_u32 s64, s75, 98304\n"
"v_add3_u32 v232, s64, v49, v51\n"
"v_xor_b32 v233, 64, v232\n"
"v_and_b32 v49, 11, v0\n"
"v_cmp_eq_u32 vcc, 0, v49\n"
"v_mov_b32 v49, 0xbf80bf80\n"
"s_nop 0\n"
"v_cndmask_b32 v32, 0, v49, vcc\n"
"v_cndmask_b32 v33, 0, v49, vcc\n"
"v_cndmask_b32 v34, 0, v49, vcc\n"
"v_cndmask_b32 v35, 0, v49, vcc\n"
"v_mov_b32 v56, 0\n"
"v_mov_b32 v57, 0\n"
"v_accvgpr_write_b32 a242, 0\n"
"v_accvgpr_write_b32 a243, 0\n"
"v_accvgpr_write_b32 a246, 0\n"
"v_accvgpr_write_b32 a247, 0\n"
"v_accvgpr_write_b32 a250, 0\n"
"v_accvgpr_write_b32 a251, 0\n"
"v_accvgpr_write_b32 a254, 0\n"
"v_accvgpr_write_b32 a255, 0\n"
"s_mul_i32 s64, s78, 14\n"
"s_sub_u32 s65, s77, s64\n"
"s_and_b32 s83, s77, 1\n"
"s_lshl_b32 s64, s78, 2\n"
"s_add_u32 s64, s64, s61\n"
"s_mul_i32 s82, s64, 14\n"
"s_add_u32 s82, s82, s65\n"
"s_lshl_b32 s82, s82, 10\n"
"s_add_u32 s82, s82, 3145728\n"
"s_lshl_b32 s6, s64, 8\n"
"s_bitcmp1_b32 s61, 0\n"
"s_waitcnt lgkmcnt(0)\n"
"s_cselect_b64 s[12:13], s[16:17], s[12:13]\n"
"buffer_load_dwordx4 v[176:179], v54, s[8:11], 0 offen\n"
"buffer_load_dwordx4 v[180:183], v54, s[8:11], 0 offen offset:512\n"
"s_add_u32 m0, s75, 0\n"
"s_mul_i32 s63, s66, 1792\n"
"buffer_load_dwordx4 v10, s[12:15], s63 offen offset:0 lds\n"
"buffer_load_dwordx4 v11, s[12:15], s63 offen offset:1024 lds\n"
"buffer_load_dwordx4 v12, s[12:15], s63 offen offset:2048 lds\n"
"buffer_load_dwordx4 v13, s[12:15], s63 offen offset:3072 lds\n"
"s_lshl_b32 s60, s62, 2\n"
"s_load_dword s60, s[34:35], s60\n"
"s_load_dwordx2 s[34:35], s[20:21], 0x0\n"
"s_add_u32 m0, s75, 98304\n"
"s_add_u32 s63, s66, s74\n"
"buffer_load_dwordx4 v6, s[8:11], s63 offen offset:0 lds\n"
"s_lshl_b32 s65, s65, 3\n"
"s_add_u32 s6, s6, s65\n"
"s_lshl_b32 s65, s76, 11\n"
"s_add_u32 s6, s6, s65\n"
"s_add_u32 s54, s6, 524288\n"
"v_lshlrev_b32 v36, 4, v44\n"
"s_sub_u32 s65, 1, s78\n"
"s_lshl_b32 s64, s76, 2\n"
"s_add_u32 s65, s65, s64\n"
"s_mul_i32 s50, s65, 229376\n"
"s_add_u32 s50, s50, s82\n"
"s_lshl_b32 s65, s65, 9\n"
"s_add_u32 s65, s65, 4096\n"
"v_add_u32 v236, s65, v36\n"
"buffer_load_dwordx4 v7, s[8:11], s63 offen offset:1024 lds\n"
"s_sub_u32 s65, 3, s78\n"
"s_lshl_b32 s64, s76, 2\n"
"s_add_u32 s65, s65, s64\n"
"s_mul_i32 s51, s65, 229376\n"
"s_add_u32 s51, s51, s82\n"
"s_lshl_b32 s65, s65, 9\n"
"s_add_u32 s65, s65, 4096\n"
"v_add_u32 v237, s65, v36\n"
"s_xor_b32 s64, s83, 0\n"
"s_lshl_b32 s65, s64, 1\n"
"s_add_u32 s65, s65, s78\n"
"s_lshl_b32 s64, s76, 2\n"
"s_add_u32 s65, s65, s64\n"
"s_mul_i32 s52, s65, 229376\n"
"buffer_load_dwordx4 v8, s[8:11], s63 offen offset:2048 lds\n"
"s_add_u32 s52, s52, s82\n"
"s_lshl_b32 s44, s65, 16\n"
"s_lshl_b32 s65, s65, 9\n"
"s_add_u32 s65, s65, 4096\n"
"v_add_u32 v238, s65, v36\n"
"s_xor_b32 s64, s83, 1\n"
"s_lshl_b32 s65, s64, 1\n"
"s_add_u32 s65, s65, s78\n"
"s_lshl_b32 s64, s76, 2\n"
"s_add_u32 s65, s65, s64\n"
"s_mul_i32 s53, s65, 229376\n"
"s_add_u32 s53, s53, s82\n"
"s_lshl_b32 s45, s65, 16\n"
"s_lshl_b32 s65, s65, 9\n"
"buffer_load_dwordx4 v9, s[8:11], s63 offen offset:3072 lds\n"
"s_add_u32 s65, s65, 4096\n"
"v_add_u32 v239, s65, v36\n"
"s_lshl_b32 s64, s61, 7\n"
"s_add_u32 s64, s64, s66\n"
"s_and_b32 s46, s64, 1023\n"
"s_add_u32 s48, s46, 32768\n"
"s_lshl_b32 s64, s61, 7\n"
"s_add_u32 s64, s64, s70\n"
"s_and_b32 s47, s64, 1023\n"
"s_add_u32 s49, s47, 32768\n"
"s_mov_b32 s58, 0x01000100\n"
"s_mov_b32 s59, 0x07060302\n"
"s_and_b32 s76, s80, 1\n"
"s_lshr_b32 s77, s80, 1\n"
"s_lshr_b32 s78, s81, 3\n"
"s_and_b32 s79, s81, 7\n"
"s_and_b32 s80, s61, 1\n"
"s_lshr_b32 s81, s61, 1\n"
"s_mul_i32 s64, s76, 917504\n"
"s_lshl_b32 s65, s61, 14\n"
"s_add_u32 s64, s64, s65\n"
"s_lshl_b32 s65, s79, 7\n"
"s_add_u32 s64, s64, s65\n"
"v_and_b32 v50, 2, v47\n"
"v_xor_b32 v49, v46, v50\n"
"v_lshlrev_b32 v49, 4, v49\n"
"v_lshl_add_u32 v49, v47, 10, v49\n"
"v_add_u32 v18, s64, v49\n"
"v_xor_b32 v49, 64, v18\n"
"v_add_u32 v19, 7168, v49\n"
"v_add_u32 v20, 63488, v18\n"
"v_add_u32 v21, 63488, v19\n"
"s_add_u32 s6, s77, s78\n"
"s_lshl_b32 s88, s6, 11\n"
"s_add_u32 s89, s88, 2048\n"
"s_cmp_eq_u32 s89, 14336\n"
"s_cselect_b32 s89, 0, s89\n"
"s_add_u32 s90, s89, 2048\n"
"s_cmp_eq_u32 s90, 14336\n"
"s_cselect_b32 s90, 0, s90\n"
"s_add_u32 s91, s90, 2048\n"
"s_cmp_eq_u32 s91, 14336\n"
"s_cselect_b32 s91, 0, s91\n"
"s_add_u32 s92, s91, 2048\n"
"s_cmp_eq_u32 s92, 14336\n"
"s_cselect_b32 s92, 0, s92\n"
"s_add_u32 s93, s92, 2048\n"
"s_cmp_eq_u32 s93, 14336\n"
"s_cselect_b32 s93, 0, s93\n"
"s_add_u32 s94, s93, 2048\n"
"s_cmp_eq_u32 s94, 14336\n"
"s_cselect_b32 s94, 0, s94\n"
"s_lshl_b32 s77, s77, 2\n"
"s_add_u32 s77, s77, s78\n"
"s_lshl_b32 s78, s77, 1\n"
"s_add_u32 s78, s78, s76\n"
"s_mul_i32 s65, s77, 458752\n"
"s_lshl_b32 s6, s76, 2\n"
"s_add_u32 s6, s6, s61\n"
"s_mul_i32 s64, s6, 14336\n"
"s_add_u32 s65, s65, s64\n"
"s_add_u32 s65, s65, 3375104\n"
"s_mul_i32 s64, s76, 229376\n"
"s_sub_u32 s65, s65, s64\n"
"s_lshl_b32 s83, s64, 1\n"
"s_sub_u32 s83, s83, 229376\n"
"v_lshlrev_b32 v49, 4, v41\n"
"v_lshl_add_u32 v49, v40, 9, v49\n"
"v_add_u32 v49, s65, v49\n"
"s_mul_i32 s64, s81, 114688\n"
"s_lshl_b32 s65, s80, 8\n"
"v_add_u32 v49, s65, v49\n"
"v_add_u32 v58, s64, v49\n"
"s_sub_u32 s64, 114688, s64\n"
"v_add_u32 v60, s64, v49\n"
"v_xor_b32 v59, 256, v58\n"
"v_xor_b32 v61, 256, v60\n"
"s_lshr_b32 s64, s77, 1\n"
"s_lshl_b32 s64, s64, 11\n"
"s_lshl_b32 s6, s6, 8\n"
"s_add_u32 s64, s64, s6\n"
"s_add_u32 s64, s64, 524288\n"
"v_min_u32 v49, 13, v0\n"
"v_lshl_add_u32 v63, v49, 3, s64\n"
"s_lshr_b32 s64, s62, 5\n"
"s_lshl_b32 s64, s64, 7\n"
"s_add_u32 s64, s64, 32768\n"
"v_min_u32 v49, 15, v0\n"
"v_lshl_add_u32 v62, v49, 3, s64\n"
"s_lshl_b32 s77, s62, 1\n"
"s_add_u32 s77, s77, s81\n"
"s_lshl_b32 s5, s77, 12\n"
"s_lshl_b32 s64, s80, 11\n"
"s_add_u32 s64, s64, s5\n"
"s_add_u32 s4, s64, 1048576\n"
"s_add_u32 s97, s64, 10485760\n"
"s_lshl_b32 s98, s80, 9\n"
"s_add_u32 s98, s98, s77\n"
"s_lshl_b32 s98, s98, 5\n"
"s_add_u32 s98, s98, 786432\n"
"s_lshl_b32 s64, s80, 3\n"
"s_add_u32 s96, s5, s64\n"
"s_add_u32 s96, s96, 4\n"
"s_mul_i32 s6, s81, 25792\n"
"s_mul_i32 s64, s80, 8320\n"
"s_add_u32 s95, s6, s64\n"
"s_lshl_b32 s64, s62, 2\n"
"s_add_u32 s64, s64, s61\n"
"s_mul_i32 s2, s64, 3072\n"
"s_xor_b32 s64, s64, 4\n"
"s_mul_i32 s3, s64, 3072\n"
"s_add_u32 s2, s2, 12582912\n"
"s_add_u32 s3, s3, 12582912\n"
"s_lshl_b32 s65, s80, 8\n"
"s_sub_u32 s82, s6, s65\n"
"v_add_u32 v50, v40, v42\n"
"v_lshlrev_b32 v50, 4, v50\n"
"v_mul_u32_u24 v51, 4160, v43\n"
"s_add_u32 s64, s82, 256\n"
"v_add3_u32 v222, s64, v50, v51\n"
"v_lshrrev_b32 v50, 1, v41\n"
"v_lshlrev_b32 v50, 2, v50\n"
"v_sub_u32 v50, v40, v50\n"
"v_lshlrev_b32 v50, 4, v50\n"
"v_and_b32 v51, 1, v0\n"
"v_mul_u32_u24 v51, 4576, v51\n"
"s_add_u32 s64, s82, 17344\n"
"v_add3_u32 v223, s64, v50, v51\n"
"s_add_u32 s6, s6, 16640\n"
"v_min_u32 v50, 27, v44\n"
"v_lshl_add_u32 v50, v50, 4, s6\n"
"v_mul_u32_u24 v51, 4576, v45\n"
"v_add_u32 v243, v50, v51\n"
"s_lshl_b32 s64, s80, 11\n"
"s_add_u32 s6, s6, s64\n"
"v_add_u32 v242, s6, v1\n"
"v_sub_u32 v50, 63, v0\n"
"v_bfe_u32 v51, v50, 1, 3\n"
"v_and_b32 v52, 48, v50\n"
"v_and_b32 v50, 1, v50\n"
"v_lshl_add_u32 v51, v50, 3, v51\n"
"v_add_lshl_u32 v51, v51, v52, 4\n"
"s_xor_b32 s64, s4, 2048\n"
"v_add_u32 v234, s64, v51\n"
"v_cmp_eq_u32 vcc, 15, v41\n"
"v_mov_b32 v52, 112\n"
"v_mov_b32 v51, 0xffffff80\n"
"v_cndmask_b32 v52, v52, -16, vcc\n"
"v_cmp_eq_u32 vcc, 1, v50\n"
"s_lshl_b32 s64, s80, 31\n"
"s_nop 0\n"
"v_cndmask_b32 v52, v52, v51, vcc\n"
"v_add_u32 v240, v52, v234\n"
"v_cmp_eq_u32 vcc, 63, v0\n"
"v_or_b32 v50, s64, v240\n"
"s_nop 0\n"
"v_cndmask_b32 v241, v240, v50, vcc\n"
"v_and_b32 v50, 1, v40\n"
"v_lshl_add_u32 v50, v45, 3, v50\n"
"v_xor_b32 v50, v50, v0\n"
"s_lshl_b32 s64, s61, 13\n"
"s_add_u32 s64, s64, 65536\n"
"v_lshl_add_u32 v244, v50, 4, s64\n"
"v_lshrrev_b32 v50, 2, v44\n"
"v_lshlrev_b32 v50, 1, v50\n"
"v_and_b32 v51, 1, v0\n"
"v_xor_b32 v50, v50, v51\n"
"v_and_b32 v51, 2, v0\n"
"v_lshlrev_b32 v51, 2, v51\n"
"v_xor_b32 v50, v50, v51\n"
"v_lshl_add_u32 v50, v43, 4, v50\n"
"v_lshl_add_u32 v50, v45, 6, v50\n"
"s_and_b32 s64, s61, 2\n"
"s_add_u32 s64, s64, s61\n"
"s_lshl_b32 s64, s64, 12\n"
"s_add_u32 s64, s64, 65536\n"
"v_lshl_add_u32 v245, v50, 4, s64\n"
"v_xor_b32 v246, 16, v245\n"
"s_xor_b32 s64, s61, 1\n"
"s_mul_i32 s64, s64, 12288\n"
"v_add_u32 v253, s64, v1\n"
"s_xor_b32 s64, s61, 2\n"
"s_mul_i32 s64, s64, 12288\n"
"v_add_u32 v254, s64, v1\n"
"s_xor_b32 s64, s61, 3\n"
"s_mul_i32 s64, s64, 12288\n"
"v_add_u32 v255, s64, v1\n"
"s_mul_i32 s64, s61, 12288\n"
"v_add_u32 v247, s64, v1\n"
"s_lshl_b32 s64, s61, 11\n"
"s_add_u32 s64, s64, 114688\n"
"v_and_b32 v50, 16, v0\n"
"v_lshlrev_b32 v50, 1, v50\n"
"v_xor_b32 v50, v50, v1\n"
"v_add_u32 v248, s64, v50\n"
"s_lshl_b32 s64, s78, 16\n"
"s_lshl_b32 s65, s61, 14\n"
"s_add_u32 s64, s64, s65\n"
"s_lshl_b32 s65, s79, 7\n"
"s_add_u32 s64, s64, s65\n"
"v_lshlrev_b32 v50, 4, v40\n"
"v_lshl_add_u32 v50, v41, 10, v50\n"
"v_add_u32 v250, s64, v50\n"
"s_lshl_b32 s6, s79, 2\n"
"s_add_u32 s6, s6, s61\n"
"s_lshl_b32 s64, s6, 16\n"
"s_lshl_b32 s65, s78, 7\n"
"s_add_u32 s64, s64, s65\n"
"s_add_u32 s64, s64, 10485760\n"
"v_lshlrev_b32 v50, 4, v46\n"
"v_lshl_add_u32 v50, v47, 12, v50\n"
"v_add_u32 v251, s64, v50\n"
"s_lshl_b32 s64, s6, 9\n"
"s_lshr_b32 s65, s78, 4\n"
"s_lshl_b32 s65, s65, 14\n"
"s_add_u32 s64, s64, s65\n"
"s_add_u32 s64, s64, 786432\n"
"v_min_u32 v49, 15, v0\n"
"v_lshl_add_u32 v252, v49, 5, s64\n"
"s_lshl_b32 s64, s78, 9\n"
"s_lshl_b32 s65, s80, 8\n"
"s_add_u32 s64, s64, s65\n"
"s_lshl_b32 s65, s81, 3\n"
"s_add_u32 s64, s64, s65\n"
"s_add_u32 s64, s64, 4096\n"
"v_lshl_add_u32 v235, v41, 4, s64\n"
"s_mov_b32 s82, 32768\n"
"s_add_u32 m0, s75, 16384\n"
"s_mul_i32 s63, s67, 1792\n"
"buffer_load_dwordx4 v10, s[12:15], s63 offen offset:0 lds\n"
"buffer_load_dwordx4 v11, s[12:15], s63 offen offset:1024 lds\n"
"buffer_load_dwordx4 v12, s[12:15], s63 offen offset:2048 lds\n"
"buffer_load_dwordx4 v13, s[12:15], s63 offen offset:3072 lds\n"
"v_cmp_eq_u32 vcc, 0, v0\n"
"v_bfrev_b32 v204, 1\n"
"v_and_b32 v205, 31, v0\n"
"v_cndmask_b32 v4, v204, v0, vcc\n"
"v_cmp_eq_u32 vcc, 0, v205\n"
"v_lshrrev_b32 v205, 1, v0\n"
"s_waitcnt lgkmcnt(0)\n"
"v_cndmask_b32 v205, v204, v205, vcc\n"
"s_mul_i32 s34, s34, 1664525\n"
"s_add_u32 s34, s34, 1013904223\n"
"s_mul_i32 s35, s35, 22695477\n"
"s_add_u32 s35, s35, 1\n"
"v_mov_b32 v2, s34\n"
"v_mov_b32 v3, s35\n"
"v_mov_b32 v203, s34\n"
"s_waitcnt vmcnt(13)\n"
"v_lshlrev_b32 v184, 16, v176\n"
"v_and_b32 v185, 0xffff0000, v176\n"
"v_lshlrev_b32 v186, 16, v177\n"
"s_add_u32 m0, s75, 114688\n"
"s_add_u32 s63, s67, s74\n"
"buffer_load_dwordx4 v6, s[8:11], s63 offen offset:0 lds\n"
"v_and_b32 v187, 0xffff0000, v177\n"
"v_lshlrev_b32 v188, 16, v178\n"
"v_and_b32 v189, 0xffff0000, v178\n"
"v_lshlrev_b32 v190, 16, v179\n"
"v_and_b32 v191, 0xffff0000, v179\n"
"s_waitcnt vmcnt(13)\n"
"v_lshlrev_b32 v192, 16, v180\n"
"v_and_b32 v193, 0xffff0000, v180\n"
"v_lshlrev_b32 v194, 16, v181\n"
"v_and_b32 v195, 0xffff0000, v181\n"
"buffer_load_dwordx4 v7, s[8:11], s63 offen offset:1024 lds\n"
"v_lshlrev_b32 v196, 16, v182\n"
"v_and_b32 v197, 0xffff0000, v182\n"
"v_lshlrev_b32 v198, 16, v183\n"
"v_and_b32 v199, 0xffff0000, v183\n"
"v_pk_add_f32 v[176:177], v[184:185], v[186:187]\n"
"v_pk_add_f32 v[178:179], v[188:189], v[190:191]\n"
"v_pk_add_f32 v[180:181], v[192:193], v[194:195]\n"
"v_pk_add_f32 v[182:183], v[196:197], v[198:199]\n"
"v_pk_add_f32 v[176:177], v[176:177], v[178:179]\n"
"buffer_load_dwordx4 v8, s[8:11], s63 offen offset:2048 lds\n"
"v_pk_add_f32 v[180:181], v[180:181], v[182:183]\n"
"v_pk_add_f32 v[176:177], v[176:177], v[180:181]\n"
"v_add_f32 v200, v176, v177\n"
"s_nop 1\n"
"v_add_f32_dpp v200, v200, v200 quad_perm:[1,0,3,2] row_mask:0xf bank_mask:0xf\n"
"s_nop 1\n"
"v_add_f32_dpp v200, v200, v200 quad_perm:[2,3,0,1] row_mask:0xf bank_mask:0xf\n"
"s_nop 1\n"
"v_add_f32_dpp v200, v200, v200 row_half_mirror row_mask:0xf bank_mask:0xf\n"
"s_nop 1\n"
"v_add_f32_dpp v200, v200, v200 row_mirror row_mask:0xf bank_mask:0xf\n"
"v_mov_b32 v201, v200\n"
"s_nop 1\n"
"v_permlane16_swap_b32 v200, v201\n"
"buffer_load_dwordx4 v9, s[8:11], s63 offen offset:3072 lds\n"
"v_add_f32 v200, v200, v201\n"
"v_mul_f32 v200, 0x3b000000, v200\n"
"v_pk_add_f32 v[184:185], v[184:185], v[200:201] op_sel_hi:[1,0] neg_lo:[0,1] neg_hi:[0,1]\n"
"v_pk_add_f32 v[186:187], v[186:187], v[200:201] op_sel_hi:[1,0] neg_lo:[0,1] neg_hi:[0,1]\n"
"v_pk_add_f32 v[188:189], v[188:189], v[200:201] op_sel_hi:[1,0] neg_lo:[0,1] neg_hi:[0,1]\n"
"v_pk_add_f32 v[190:191], v[190:191], v[200:201] op_sel_hi:[1,0] neg_lo:[0,1] neg_hi:[0,1]\n"
"v_pk_add_f32 v[192:193], v[192:193], v[200:201] op_sel_hi:[1,0] neg_lo:[0,1] neg_hi:[0,1]\n"
"v_pk_add_f32 v[194:195], v[194:195], v[200:201] op_sel_hi:[1,0] neg_lo:[0,1] neg_hi:[0,1]\n"
"v_pk_add_f32 v[196:197], v[196:197], v[200:201] op_sel_hi:[1,0] neg_lo:[0,1] neg_hi:[0,1]\n"
"s_add_u32 m0, s75, 32768\n"
"s_mul_i32 s63, s68, 1792\n"
"buffer_load_dwordx4 v10, s[12:15], s63 offen offset:0 lds\n"
"v_pk_add_f32 v[198:199], v[198:199], v[200:201] op_sel_hi:[1,0] neg_lo:[0,1] neg_hi:[0,1]\n"
"v_pk_mul_f32 v[176:177], v[184:185], v[184:185]\n"
"v_pk_mul_f32 v[178:179], v[186:187], v[186:187]\n"
"v_pk_fma_f32 v[176:177], v[188:189], v[188:189], v[176:177]\n"
"v_pk_fma_f32 v[178:179], v[190:191], v[190:191], v[178:179]\n"
"v_pk_fma_f32 v[176:177], v[192:193], v[192:193], v[176:177]\n"
"v_pk_fma_f32 v[178:179], v[194:195], v[194:195], v[178:179]\n"
"v_pk_fma_f32 v[176:177], v[196:197], v[196:197], v[176:177]\n"
"v_pk_fma_f32 v[178:179], v[198:199], v[198:199], v[178:179]\n"
"buffer_load_dwordx4 v11, s[12:15], s63 offen offset:1024 lds\n"
"v_pk_add_f32 v[176:177], v[176:177], v[178:179]\n"
"v_add_f32 v176, v176, v177\n"
"s_nop 1\n"
"v_add_f32_dpp v176, v176, v176 quad_perm:[1,0,3,2] row_mask:0xf bank_mask:0xf\n"
"s_nop 1\n"
"v_add_f32_dpp v176, v176, v176 quad_perm:[2,3,0,1] row_mask:0xf bank_mask:0xf\n"
"s_nop 1\n"
"v_add_f32_dpp v176, v176, v176 row_half_mirror row_mask:0xf bank_mask:0xf\n"
"s_nop 1\n"
"v_add_f32_dpp v176, v176, v176 row_mirror row_mask:0xf bank_mask:0xf\n"
"v_mov_b32 v177, v176\n"
"s_nop 1\n"
"v_permlane16_swap_b32 v176, v177\n"
"v_add_f32 v176, v176, v177\n"
"buffer_load_dwordx4 v12, s[12:15], s63 offen offset:2048 lds\n"
"v_mul_f32 v176, 0x3b000000, v176\n"
"v_cvt_pk_bf16_f32 v176, v176, v176\n"
"v_lshlrev_b32 v176, 16, v176\n"
"v_add_f32 v176, 0x3727c5ac, v176\n"
"v_cvt_pk_bf16_f32 v176, v176, v176\n"
"v_lshlrev_b32 v176, 16, v176\n"
"v_sqrt_f32 v176, v176\n"
"s_nop 0\n"
"v_cvt_pk_bf16_f32 v202, v200, v176\n"
"buffer_load_dwordx4 v13, s[12:15], s63 offen offset:3072 lds\n"
"s_waitcnt vmcnt(12)\n"
"s_barrier\n"
"ds_read_b64_tr_b16 a[0:1], v5 offset:0\n"
"ds_read_b64_tr_b16 a[2:3], v5 offset:512\n"
"ds_read_b128 v[144:147], v14 offset:32768\n"
"ds_read_b64_tr_b16 a[8:9], v5 offset:8192\n"
"ds_read_b64_tr_b16 a[10:11], v5 offset:8704\n"
"ds_read_b128 v[148:151], v15 offset:32768\n"
"ds_read_b64_tr_b16 a[4:5], v5 offset:4096\n"
"ds_read_b64_tr_b16 a[6:7], v5 offset:4608\n"
"ds_read_b64_tr_b16 a[12:13], v5 offset:12288\n"
"ds_read_b64_tr_b16 a[14:15], v5 offset:12800\n"
"ds_read_b128 v[152:155], v14 offset:34816\n"
"ds_read_b128 v[156:159], v15 offset:34816\n"
"ds_read_b128 v[160:163], v14 offset:36864\n"
"ds_read_b128 v[164:167], v15 offset:36864\n"
"ds_read_b128 v[168:171], v14 offset:38912\n"
"s_add_u32 m0, s75, 65536\n"
"s_add_u32 s63, s68, s74\n"
"s_waitcnt lgkmcnt(12)\n"
"v_mfma_f32_16x16x32_bf16 v[64:67], a[0:3], v[144:147], 0\n"
"s_lshl_b32 s64, s81, 4\n"
"s_waitcnt lgkmcnt(9)\n"
"v_mfma_f32_16x16x32_bf16 v[64:67], a[8:11], v[148:151], v[64:67]\n"
"s_lshr_b32 s60, s60, s64\n"
"s_waitcnt lgkmcnt(7)\n"
"v_mfma_f32_16x16x32_bf16 v[68:71], a[4:7], v[144:147], 0\n"
"s_and_b32 s60, s60, 0xffff\n"
"s_waitcnt lgkmcnt(5)\n"
"v_mfma_f32_16x16x32_bf16 v[68:71], a[12:15], v[148:151], v[68:71]\n"
"s_lshl_b32 s64, s80, 8\n"
"s_waitcnt lgkmcnt(4)\n"
"v_mfma_f32_16x16x32_bf16 v[72:75], a[0:3], v[152:155], 0\n"
"buffer_store_dwordx2 v[202:203], v205, s[20:23], s99 offen sc1\n"
"buffer_load_dwordx4 v6, s[8:11], s63 offen offset:0 lds\n"
"s_waitcnt lgkmcnt(3)\n"
"v_mfma_f32_16x16x32_bf16 v[72:75], a[8:11], v[156:159], v[72:75]\n"
"ds_read_b128 v[172:175], v15 offset:38912\n"
"ds_read_b128 v[144:147], v14 offset:40960\n"
"v_mfma_f32_16x16x32_bf16 v[76:79], a[4:7], v[152:155], 0\n"
"ds_read_b128 v[148:151], v15 offset:40960\n"
"buffer_load_dwordx4 v7, s[8:11], s63 offen offset:1024 lds\n"
"v_mfma_f32_16x16x32_bf16 v[76:79], a[12:15], v[156:159], v[76:79]\n"
"ds_read_b128 v[152:155], v14 offset:43008\n"
"s_sub_u32 s64, 196, s64\n"
"v_mfma_f32_16x16x32_bf16 v[24:27], v[32:35], a[0:3], 0\n"
"v_and_b32 v51, 48, v0\n"
"v_lshl_add_u32 v50, v41, 2, v51\n"
"v_add_u32 v50, s64, v50\n"
"v_mfma_f32_16x16x32_bf16 v[24:27], v[32:35], a[8:11], v[24:27]\n"
"v_mov_b32 v51, s60\n"
"buffer_load_dwordx4 v8, s[8:11], s63 offen offset:2048 lds\n"
"s_waitcnt lgkmcnt(6)\n"
"v_mfma_f32_16x16x32_bf16 v[80:83], a[0:3], v[160:163], 0\n"
"ds_read_b128 v[156:159], v15 offset:43008\n"
"v_add_u32 v228, 0, v50\n"
"v_cmp_eq_u32 vcc, 0, v228\n"
"s_waitcnt lgkmcnt(6)\n"
"v_mfma_f32_16x16x32_bf16 v[80:83], a[8:11], v[164:167], v[80:83]\n"
"v_min_u32 v228, 0x7fffffff, v228\n"
"s_nop 0\n"
"v_cndmask_b32 v224, 0, v51, vcc\n"
"v_mfma_f32_16x16x32_bf16 v[84:87], a[4:7], v[160:163], 0\n"
"ds_read_b128 v[160:163], v14 offset:45056\n"
"v_add_u32 v229, 4, v50\n"
"v_cmp_eq_u32 vcc, 0, v229\n"
"v_mfma_f32_16x16x32_bf16 v[84:87], a[12:15], v[164:167], v[84:87]\n"
"v_min_u32 v229, 0x7fffffff, v229\n"
"s_nop 0\n"
"v_cndmask_b32 v225, 0, v51, vcc\n"
"s_waitcnt lgkmcnt(4)\n"
"v_mfma_f32_16x16x32_bf16 v[88:91], a[0:3], v[168:171], 0\n"
"ds_read_b128 v[164:167], v15 offset:45056\n"
"buffer_load_dwordx4 v9, s[8:11], s63 offen offset:3072 lds\n"
"s_add_u32 m0, s75, 49152\n"
"v_mfma_f32_16x16x32_bf16 v[88:91], a[8:11], v[172:175], v[88:91]\n"
"s_mul_i32 s63, s69, 1792\n"
"buffer_load_dwordx4 v10, s[12:15], s63 offen offset:0 lds\n"
"v_mfma_f32_16x16x32_bf16 v[92:95], a[4:7], v[168:171], 0\n"
"ds_read_b128 v[168:171], v14 offset:47104\n"
"v_add_u32 v230, 8, v50\n"
"v_cmp_eq_u32 vcc, 0, v230\n"
"v_mfma_f32_16x16x32_bf16 v[92:95], a[12:15], v[172:175], v[92:95]\n"
"v_min_u32 v230, 0x7fffffff, v230\n"
"s_nop 0\n"
"v_cndmask_b32 v226, 0, v51, vcc\n"
"v_mfma_f32_16x16x32_bf16 v[28:31], v[32:35], a[4:7], 0\n"
"buffer_load_dwordx4 v11, s[12:15], s63 offen offset:1024 lds\n"
"v_add_u32 v231, 12, v50\n"
"v_mfma_f32_16x16x32_bf16 v[28:31], v[32:35], a[12:15], v[28:31]\n"
"v_cmp_eq_u32 vcc, 0, v231\n"
"v_min_u32 v231, 0x7fffffff, v231\n"
"s_nop 0\n"
"v_mfma_f32_16x16x32_bf16 v[96:99], a[0:3], v[144:147], 0\n"
"ds_read_b128 v[172:175], v15 offset:47104\n"
"v_cndmask_b32 v227, 0, v51, vcc\n"
"s_waitcnt lgkmcnt(3)\n"
"v_mfma_f32_16x16x32_bf16 v[96:99], a[8:11], v[148:151], v[96:99]\n"
"v_mfma_f32_16x16x32_bf16 v[100:103], a[4:7], v[144:147], 0\n"
"buffer_load_dwordx4 v12, s[12:15], s63 offen offset:2048 lds\n"
"v_mfma_f32_16x16x32_bf16 v[100:103], a[12:15], v[148:151], v[100:103]\n"
"v_mfma_f32_16x16x32_bf16 v[104:107], a[0:3], v[152:155], 0\n"
"v_mfma_f32_16x16x32_bf16 v[104:107], a[8:11], v[156:159], v[104:107]\n"
"v_mfma_f32_16x16x32_bf16 v[108:111], a[4:7], v[152:155], 0\n"
"s_waitcnt vmcnt(12) lgkmcnt(0)\n"
"s_barrier\n"
"buffer_load_dwordx4 v13, s[12:15], s63 offen offset:3072 lds\n"
"v_mfma_f32_16x16x32_bf16 v[108:111], a[12:15], v[156:159], v[108:111]\n"
"ds_read_b64_tr_b16 a[16:17], v5 offset:16384\n"
"ds_read_b64_tr_b16 a[18:19], v5 offset:16896\n"
"v_mfma_f32_16x16x32_bf16 v[112:115], a[0:3], v[160:163], 0\n"
"ds_read_b128 v[144:147], v14 offset:49152\n"
"ds_read_b64_tr_b16 a[24:25], v5 offset:24576\n"
"v_mfma_f32_16x16x32_bf16 v[112:115], a[8:11], v[164:167], v[112:115]\n"
"ds_read_b64_tr_b16 a[26:27], v5 offset:25088\n"
"ds_read_b128 v[148:151], v15 offset:49152\n"
"v_mfma_f32_16x16x32_bf16 v[116:119], a[4:7], v[160:163], 0\n"
"ds_read_b64_tr_b16 a[20:21], v5 offset:20480\n"
"ds_read_b64_tr_b16 a[22:23], v5 offset:20992\n"
"v_mfma_f32_16x16x32_bf16 v[116:119], a[12:15], v[164:167], v[116:119]\n"
"ds_read_b64_tr_b16 a[28:29], v5 offset:28672\n"
"ds_read_b64_tr_b16 a[30:31], v5 offset:29184\n"
"v_mfma_f32_16x16x32_bf16 v[120:123], a[0:3], v[168:171], 0\n"
"ds_read_b128 v[152:155], v14 offset:51200\n"
"s_add_u32 m0, s75, 81920\n"
"v_mfma_f32_16x16x32_bf16 v[120:123], a[8:11], v[172:175], v[120:123]\n"
"ds_read_b128 v[156:159], v15 offset:51200\n"
"s_add_u32 s63, s69, s74\n"
"v_mfma_f32_16x16x32_bf16 v[124:127], a[4:7], v[168:171], 0\n"
"ds_read_b128 v[160:163], v14 offset:53248\n"
"v_mfma_f32_16x16x32_bf16 v[124:127], a[12:15], v[172:175], v[124:127]\n"
"ds_read_b128 v[164:167], v15 offset:53248\n"
"s_waitcnt lgkmcnt(11)\n"
"v_mfma_f32_16x16x32_bf16 v[64:67], a[16:19], v[144:147], v[64:67]\n"
"ds_read_b128 v[168:171], v14 offset:55296\n"
"s_waitcnt lgkmcnt(9)\n"
"v_mfma_f32_16x16x32_bf16 v[64:67], a[24:27], v[148:151], v[64:67]\n"
"ds_read_b128 v[172:175], v15 offset:55296\n"
"buffer_load_dwordx4 v6, s[8:11], s63 offen offset:0 lds\n"
"s_waitcnt lgkmcnt(8)\n"
"v_mfma_f32_16x16x32_bf16 v[68:71], a[20:23], v[144:147], v[68:71]\n"
"ds_read_b128 v[144:147], v14 offset:57344\n"
"s_waitcnt lgkmcnt(7)\n"
"v_mfma_f32_16x16x32_bf16 v[68:71], a[28:31], v[148:151], v[68:71]\n"
"s_waitcnt lgkmcnt(6)\n"
"v_mfma_f32_16x16x32_bf16 v[72:75], a[16:19], v[152:155], v[72:75]\n"
"ds_read_b128 v[148:151], v15 offset:57344\n"
"buffer_load_dwordx4 v7, s[8:11], s63 offen offset:1024 lds\n"
"s_waitcnt lgkmcnt(6)\n"
"v_mfma_f32_16x16x32_bf16 v[72:75], a[24:27], v[156:159], v[72:75]\n"
"buffer_load_dwordx4 v8, s[8:11], s63 offen offset:2048 lds\n"
"v_mfma_f32_16x16x32_bf16 v[76:79], a[20:23], v[152:155], v[76:79]\n"
"ds_read_b128 v[152:155], v14 offset:59392\n"
"v_mfma_f32_16x16x32_bf16 v[76:79], a[28:31], v[156:159], v[76:79]\n"
"v_mfma_f32_16x16x32_bf16 v[24:27], v[32:35], a[16:19], v[24:27]\n"
"v_mfma_f32_16x16x32_bf16 v[24:27], v[32:35], a[24:27], v[24:27]\n"
"s_waitcnt lgkmcnt(4)\n"
"v_mfma_f32_16x16x32_bf16 v[80:83], a[16:19], v[160:163], v[80:83]\n"
"ds_read_b128 v[156:159], v15 offset:59392\n"
"buffer_load_dwordx4 v9, s[8:11], s63 offen offset:3072 lds\n"
"v_mfma_f32_16x16x32_bf16 v[80:83], a[24:27], v[164:167], v[80:83]\n"
"s_add_u32 m0, s75, 0\n"
"s_mul_i32 s63, s70, 1792\n"
"v_mfma_f32_16x16x32_bf16 v[84:87], a[20:23], v[160:163], v[84:87]\n"
"ds_read_b128 v[160:163], v14 offset:61440\n"
"v_mfma_f32_16x16x32_bf16 v[84:87], a[28:31], v[164:167], v[84:87]\n"
"buffer_load_dwordx4 v10, s[12:15], s63 offen offset:0 lds\n"
"v_mfma_f32_16x16x32_bf16 v[88:91], a[16:19], v[168:171], v[88:91]\n"
"ds_read_b128 v[164:167], v15 offset:61440\n"
"s_waitcnt lgkmcnt(4)\n"
"v_mfma_f32_16x16x32_bf16 v[88:91], a[24:27], v[172:175], v[88:91]\n"
"v_mfma_f32_16x16x32_bf16 v[92:95], a[20:23], v[168:171], v[92:95]\n"
"ds_read_b128 v[168:171], v14 offset:63488\n"
"v_mfma_f32_16x16x32_bf16 v[92:95], a[28:31], v[172:175], v[92:95]\n"
"buffer_load_dwordx4 v11, s[12:15], s63 offen offset:1024 lds\n"
"v_mfma_f32_16x16x32_bf16 v[28:31], v[32:35], a[20:23], v[28:31]\n"
"v_mfma_f32_16x16x32_bf16 v[28:31], v[32:35], a[28:31], v[28:31]\n"
"v_mfma_f32_16x16x32_bf16 v[96:99], a[16:19], v[144:147], v[96:99]\n"
"ds_read_b128 v[172:175], v15 offset:63488\n"
"v_mfma_f32_16x16x32_bf16 v[96:99], a[24:27], v[148:151], v[96:99]\n"
"buffer_load_dwordx4 v12, s[12:15], s63 offen offset:2048 lds\n"
"v_mfma_f32_16x16x32_bf16 v[100:103], a[20:23], v[144:147], v[100:103]\n"
"v_mfma_f32_16x16x32_bf16 v[100:103], a[28:31], v[148:151], v[100:103]\n"
"s_waitcnt lgkmcnt(2)\n"
"v_mfma_f32_16x16x32_bf16 v[104:107], a[16:19], v[152:155], v[104:107]\n"
"v_mfma_f32_16x16x32_bf16 v[104:107], a[24:27], v[156:159], v[104:107]\n"
"buffer_load_dwordx4 v13, s[12:15], s63 offen offset:3072 lds\n"
"v_mfma_f32_16x16x32_bf16 v[108:111], a[20:23], v[152:155], v[108:111]\n"
"s_waitcnt vmcnt(12) lgkmcnt(0)\n"
"s_barrier\n"
"v_mfma_f32_16x16x32_bf16 v[108:111], a[28:31], v[156:159], v[108:111]\n"
"ds_read_b64_tr_b16 a[32:33], v5 offset:32768\n"
"ds_read_b64_tr_b16 a[34:35], v5 offset:33280\n"
"v_mfma_f32_16x16x32_bf16 v[112:115], a[16:19], v[160:163], v[112:115]\n"
"ds_read_b128 v[144:147], v14 offset:0\n"
"ds_read_b64_tr_b16 a[40:41], v5 offset:40960\n"
"v_mfma_f32_16x16x32_bf16 v[112:115], a[24:27], v[164:167], v[112:115]\n"
"ds_read_b64_tr_b16 a[42:43], v5 offset:41472\n"
"ds_read_b128 v[148:151], v15 offset:0\n"
"v_mfma_f32_16x16x32_bf16 v[116:119], a[20:23], v[160:163], v[116:119]\n"
"ds_read_b64_tr_b16 a[36:37], v5 offset:36864\n"
"ds_read_b64_tr_b16 a[38:39], v5 offset:37376\n"
"v_mfma_f32_16x16x32_bf16 v[116:119], a[28:31], v[164:167], v[116:119]\n"
"ds_read_b64_tr_b16 a[44:45], v5 offset:45056\n"
"ds_read_b64_tr_b16 a[46:47], v5 offset:45568\n"
"v_mfma_f32_16x16x32_bf16 v[120:123], a[16:19], v[168:171], v[120:123]\n"
"ds_read_b128 v[152:155], v14 offset:2048\n"
"s_add_u32 m0, s75, 98304\n"
"v_mfma_f32_16x16x32_bf16 v[120:123], a[24:27], v[172:175], v[120:123]\n"
"ds_read_b128 v[156:159], v15 offset:2048\n"
"s_add_u32 s63, s70, s74\n"
"v_mfma_f32_16x16x32_bf16 v[124:127], a[20:23], v[168:171], v[124:127]\n"
"ds_read_b128 v[160:163], v14 offset:4096\n"
"v_mfma_f32_16x16x32_bf16 v[124:127], a[28:31], v[172:175], v[124:127]\n"
"ds_read_b128 v[164:167], v15 offset:4096\n"
"s_waitcnt lgkmcnt(11)\n"
"v_mfma_f32_16x16x32_bf16 v[64:67], a[32:35], v[144:147], v[64:67]\n"
"ds_read_b128 v[168:171], v14 offset:6144\n"
"s_waitcnt lgkmcnt(9)\n"
"v_mfma_f32_16x16x32_bf16 v[64:67], a[40:43], v[148:151], v[64:67]\n"
"ds_read_b128 v[172:175], v15 offset:6144\n"
"buffer_load_dwordx4 v6, s[8:11], s63 offen offset:0 lds\n"
"s_waitcnt lgkmcnt(8)\n"
"v_mfma_f32_16x16x32_bf16 v[68:71], a[36:39], v[144:147], v[68:71]\n"
"ds_read_b128 v[144:147], v14 offset:8192\n"
"s_waitcnt lgkmcnt(7)\n"
"v_mfma_f32_16x16x32_bf16 v[68:71], a[44:47], v[148:151], v[68:71]\n"
"s_waitcnt lgkmcnt(6)\n"
"v_mfma_f32_16x16x32_bf16 v[72:75], a[32:35], v[152:155], v[72:75]\n"
"ds_read_b128 v[148:151], v15 offset:8192\n"
"buffer_load_dwordx4 v7, s[8:11], s63 offen offset:1024 lds\n"
"s_waitcnt lgkmcnt(6)\n"
"v_mfma_f32_16x16x32_bf16 v[72:75], a[40:43], v[156:159], v[72:75]\n"
"buffer_load_dwordx4 v8, s[8:11], s63 offen offset:2048 lds\n"
"v_mfma_f32_16x16x32_bf16 v[76:79], a[36:39], v[152:155], v[76:79]\n"
"ds_read_b128 v[152:155], v14 offset:10240\n"
"v_mfma_f32_16x16x32_bf16 v[76:79], a[44:47], v[156:159], v[76:79]\n"
"v_mfma_f32_16x16x32_bf16 v[24:27], v[32:35], a[32:35], v[24:27]\n"
"v_mfma_f32_16x16x32_bf16 v[24:27], v[32:35], a[40:43], v[24:27]\n"
"s_waitcnt lgkmcnt(4)\n"
"v_mfma_f32_16x16x32_bf16 v[80:83], a[32:35], v[160:163], v[80:83]\n"
"ds_read_b128 v[156:159], v15 offset:10240\n"
"buffer_load_dwordx4 v9, s[8:11], s63 offen offset:3072 lds\n"
"v_mfma_f32_16x16x32_bf16 v[80:83], a[40:43], v[164:167], v[80:83]\n"
"s_add_u32 m0, s75, 16384\n"
"s_mul_i32 s63, s71, 1792\n"
"v_mfma_f32_16x16x32_bf16 v[84:87], a[36:39], v[160:163], v[84:87]\n"
"ds_read_b128 v[160:163], v14 offset:12288\n"
"v_mfma_f32_16x16x32_bf16 v[84:87], a[44:47], v[164:167], v[84:87]\n"
"buffer_load_dwordx4 v10, s[12:15], s63 offen offset:0 lds\n"
"v_mfma_f32_16x16x32_bf16 v[88:91], a[32:35], v[168:171], v[88:91]\n"
"ds_read_b128 v[164:167], v15 offset:12288\n"
"s_waitcnt lgkmcnt(4)\n"
"v_mfma_f32_16x16x32_bf16 v[88:91], a[40:43], v[172:175], v[88:91]\n"
"v_mfma_f32_16x16x32_bf16 v[92:95], a[36:39], v[168:171], v[92:95]\n"
"ds_read_b128 v[168:171], v14 offset:14336\n"
"v_mfma_f32_16x16x32_bf16 v[92:95], a[44:47], v[172:175], v[92:95]\n"
"buffer_load_dwordx4 v11, s[12:15], s63 offen offset:1024 lds\n"
"v_mfma_f32_16x16x32_bf16 v[28:31], v[32:35], a[36:39], v[28:31]\n"
"v_mfma_f32_16x16x32_bf16 v[28:31], v[32:35], a[44:47], v[28:31]\n"
"v_mfma_f32_16x16x32_bf16 v[96:99], a[32:35], v[144:147], v[96:99]\n"
"ds_read_b128 v[172:175], v15 offset:14336\n"
"v_mfma_f32_16x16x32_bf16 v[96:99], a[40:43], v[148:151], v[96:99]\n"
"buffer_load_dwordx4 v12, s[12:15], s63 offen offset:2048 lds\n"
"v_mfma_f32_16x16x32_bf16 v[100:103], a[36:39], v[144:147], v[100:103]\n"
"v_mfma_f32_16x16x32_bf16 v[100:103], a[44:47], v[148:151], v[100:103]\n"
"s_waitcnt lgkmcnt(2)\n"
"v_mfma_f32_16x16x32_bf16 v[104:107], a[32:35], v[152:155], v[104:107]\n"
"v_mfma_f32_16x16x32_bf16 v[104:107], a[40:43], v[156:159], v[104:107]\n"
"buffer_load_dwordx4 v13, s[12:15], s63 offen offset:3072 lds\n"
"v_mfma_f32_16x16x32_bf16 v[108:111], a[36:39], v[152:155], v[108:111]\n"
"s_waitcnt vmcnt(12) lgkmcnt(0)\n"
"s_barrier\n"
"v_mfma_f32_16x16x32_bf16 v[108:111], a[44:47], v[156:159], v[108:111]\n"
"ds_read_b64_tr_b16 a[48:49], v5 offset:49152\n"
"ds_read_b64_tr_b16 a[50:51], v5 offset:49664\n"
"v_mfma_f32_16x16x32_bf16 v[112:115], a[32:35], v[160:163], v[112:115]\n"
"ds_read_b128 v[144:147], v14 offset:16384\n"
"ds_read_b64_tr_b16 a[56:57], v5 offset:57344\n"
"v_mfma_f32_16x16x32_bf16 v[112:115], a[40:43], v[164:167], v[112:115]\n"
"ds_read_b64_tr_b16 a[58:59], v5 offset:57856\n"
"ds_read_b128 v[148:151], v15 offset:16384\n"
"v_mfma_f32_16x16x32_bf16 v[116:119], a[36:39], v[160:163], v[116:119]\n"
"ds_read_b64_tr_b16 a[52:53], v5 offset:53248\n"
"ds_read_b64_tr_b16 a[54:55], v5 offset:53760\n"
"v_mfma_f32_16x16x32_bf16 v[116:119], a[44:47], v[164:167], v[116:119]\n"
"ds_read_b64_tr_b16 a[60:61], v5 offset:61440\n"
"ds_read_b64_tr_b16 a[62:63], v5 offset:61952\n"
"v_mfma_f32_16x16x32_bf16 v[120:123], a[32:35], v[168:171], v[120:123]\n"
"ds_read_b128 v[152:155], v14 offset:18432\n"
"s_add_u32 m0, s75, 114688\n"
"v_mfma_f32_16x16x32_bf16 v[120:123], a[40:43], v[172:175], v[120:123]\n"
"ds_read_b128 v[156:159], v15 offset:18432\n"
"s_add_u32 s63, s71, s74\n"
"v_mfma_f32_16x16x32_bf16 v[124:127], a[36:39], v[168:171], v[124:127]\n"
"ds_read_b128 v[160:163], v14 offset:20480\n"
"v_mfma_f32_16x16x32_bf16 v[124:127], a[44:47], v[172:175], v[124:127]\n"
"ds_read_b128 v[164:167], v15 offset:20480\n"
"s_waitcnt lgkmcnt(11)\n"
"v_mfma_f32_16x16x32_bf16 v[64:67], a[48:51], v[144:147], v[64:67]\n"
"ds_read_b128 v[168:171], v14 offset:22528\n"
"s_waitcnt lgkmcnt(9)\n"
"v_mfma_f32_16x16x32_bf16 v[64:67], a[56:59], v[148:151], v[64:67]\n"
"ds_read_b128 v[172:175], v15 offset:22528\n"
"buffer_load_dwordx4 v6, s[8:11], s63 offen offset:0 lds\n"
"s_waitcnt lgkmcnt(8)\n"
"v_mfma_f32_16x16x32_bf16 v[68:71], a[52:55], v[144:147], v[68:71]\n"
"ds_read_b128 v[144:147], v14 offset:24576\n"
"s_waitcnt lgkmcnt(7)\n"
"v_mfma_f32_16x16x32_bf16 v[68:71], a[60:63], v[148:151], v[68:71]\n"
"s_waitcnt lgkmcnt(6)\n"
"v_mfma_f32_16x16x32_bf16 v[72:75], a[48:51], v[152:155], v[72:75]\n"
"ds_read_b128 v[148:151], v15 offset:24576\n"
"buffer_load_dwordx4 v7, s[8:11], s63 offen offset:1024 lds\n"
"s_waitcnt lgkmcnt(6)\n"
"v_mfma_f32_16x16x32_bf16 v[72:75], a[56:59], v[156:159], v[72:75]\n"
"buffer_load_dwordx4 v8, s[8:11], s63 offen offset:2048 lds\n"
"v_mfma_f32_16x16x32_bf16 v[76:79], a[52:55], v[152:155], v[76:79]\n"
"ds_read_b128 v[152:155], v14 offset:26624\n"
"v_mfma_f32_16x16x32_bf16 v[76:79], a[60:63], v[156:159], v[76:79]\n"
"v_mfma_f32_16x16x32_bf16 v[24:27], v[32:35], a[48:51], v[24:27]\n"
"v_mfma_f32_16x16x32_bf16 v[24:27], v[32:35], a[56:59], v[24:27]\n"
"s_waitcnt lgkmcnt(4)\n"
"v_mfma_f32_16x16x32_bf16 v[80:83], a[48:51], v[160:163], v[80:83]\n"
"ds_read_b128 v[156:159], v15 offset:26624\n"
"buffer_load_dwordx4 v9, s[8:11], s63 offen offset:3072 lds\n"
"v_mfma_f32_16x16x32_bf16 v[80:83], a[56:59], v[164:167], v[80:83]\n"
"s_add_u32 m0, s75, 32768\n"
"s_mul_i32 s63, s72, 1792\n"
"v_mfma_f32_16x16x32_bf16 v[84:87], a[52:55], v[160:163], v[84:87]\n"
"ds_read_b128 v[160:163], v14 offset:28672\n"
"v_mfma_f32_16x16x32_bf16 v[84:87], a[60:63], v[164:167], v[84:87]\n"
"buffer_load_dwordx4 v10, s[12:15], s63 offen offset:0 lds\n"
"v_mfma_f32_16x16x32_bf16 v[88:91], a[48:51], v[168:171], v[88:91]\n"
"ds_read_b128 v[164:167], v15 offset:28672\n"
"s_waitcnt lgkmcnt(4)\n"
"v_mfma_f32_16x16x32_bf16 v[88:91], a[56:59], v[172:175], v[88:91]\n"
"v_mfma_f32_16x16x32_bf16 v[92:95], a[52:55], v[168:171], v[92:95]\n"
"ds_read_b128 v[168:171], v14 offset:30720\n"
"v_mfma_f32_16x16x32_bf16 v[92:95], a[60:63], v[172:175], v[92:95]\n"
"buffer_load_dwordx4 v11, s[12:15], s63 offen offset:1024 lds\n"
"v_mfma_f32_16x16x32_bf16 v[28:31], v[32:35], a[52:55], v[28:31]\n"
"v_mfma_f32_16x16x32_bf16 v[28:31], v[32:35], a[60:63], v[28:31]\n"
"v_mfma_f32_16x16x32_bf16 v[96:99], a[48:51], v[144:147], v[96:99]\n"
"ds_read_b128 v[172:175], v15 offset:30720\n"
"v_mfma_f32_16x16x32_bf16 v[96:99], a[56:59], v[148:151], v[96:99]\n"
"buffer_load_dwordx4 v12, s[12:15], s63 offen offset:2048 lds\n"
"v_mfma_f32_16x16x32_bf16 v[100:103], a[52:55], v[144:147], v[100:103]\n"
"v_mfma_f32_16x16x32_bf16 v[100:103], a[60:63], v[148:151], v[100:103]\n"
"s_waitcnt lgkmcnt(2)\n"
"v_mfma_f32_16x16x32_bf16 v[104:107], a[48:51], v[152:155], v[104:107]\n"
"v_mfma_f32_16x16x32_bf16 v[104:107], a[56:59], v[156:159], v[104:107]\n"
"buffer_load_dwordx4 v13, s[12:15], s63 offen offset:3072 lds\n"
"v_mfma_f32_16x16x32_bf16 v[108:111], a[52:55], v[152:155], v[108:111]\n"
"s_waitcnt vmcnt(12) lgkmcnt(0)\n"
"s_barrier\n"
"v_mfma_f32_16x16x32_bf16 v[108:111], a[60:63], v[156:159], v[108:111]\n"
"ds_read_b64_tr_b16 a[64:65], v5 offset:0\n"
"ds_read_b64_tr_b16 a[66:67], v5 offset:512\n"
"v_mfma_f32_16x16x32_bf16 v[112:115], a[48:51], v[160:163], v[112:115]\n"
"ds_read_b128 v[144:147], v14 offset:32768\n"
"ds_read_b64_tr_b16 a[72:73], v5 offset:8192\n"
"v_mfma_f32_16x16x32_bf16 v[112:115], a[56:59], v[164:167], v[112:115]\n"
"ds_read_b64_tr_b16 a[74:75], v5 offset:8704\n"
"ds_read_b128 v[148:151], v15 offset:32768\n"
"v_mfma_f32_16x16x32_bf16 v[116:119], a[52:55], v[160:163], v[116:119]\n"
"ds_read_b64_tr_b16 a[68:69], v5 offset:4096\n"
"ds_read_b64_tr_b16 a[70:71], v5 offset:4608\n"
"v_mfma_f32_16x16x32_bf16 v[116:119], a[60:63], v[164:167], v[116:119]\n"
"ds_read_b64_tr_b16 a[76:77], v5 offset:12288\n"
"ds_read_b64_tr_b16 a[78:79], v5 offset:12800\n"
"v_mfma_f32_16x16x32_bf16 v[120:123], a[48:51], v[168:171], v[120:123]\n"
"ds_read_b128 v[152:155], v14 offset:34816\n"
"s_add_u32 m0, s75, 65536\n"
"v_mfma_f32_16x16x32_bf16 v[120:123], a[56:59], v[172:175], v[120:123]\n"
"ds_read_b128 v[156:159], v15 offset:34816\n"
"s_add_u32 s63, s72, s74\n"
"v_mfma_f32_16x16x32_bf16 v[124:127], a[52:55], v[168:171], v[124:127]\n"
"ds_read_b128 v[160:163], v14 offset:36864\n"
"v_mfma_f32_16x16x32_bf16 v[124:127], a[60:63], v[172:175], v[124:127]\n"
"ds_read_b128 v[164:167], v15 offset:36864\n"
"s_waitcnt lgkmcnt(11)\n"
"v_mfma_f32_16x16x32_bf16 v[64:67], a[64:67], v[144:147], v[64:67]\n"
"ds_read_b128 v[168:171], v14 offset:38912\n"
"s_waitcnt lgkmcnt(9)\n"
"v_mfma_f32_16x16x32_bf16 v[64:67], a[72:75], v[148:151], v[64:67]\n"
"ds_read_b128 v[172:175], v15 offset:38912\n"
"buffer_load_dwordx4 v6, s[8:11], s63 offen offset:0 lds\n"
"s_waitcnt lgkmcnt(8)\n"
"v_mfma_f32_16x16x32_bf16 v[68:71], a[68:71], v[144:147], v[68:71]\n"
"ds_read_b128 v[144:147], v14 offset:40960\n"
"s_waitcnt lgkmcnt(7)\n"
"v_mfma_f32_16x16x32_bf16 v[68:71], a[76:79], v[148:151], v[68:71]\n"
"s_waitcnt lgkmcnt(6)\n"
"v_mfma_f32_16x16x32_bf16 v[72:75], a[64:67], v[152:155], v[72:75]\n"
"ds_read_b128 v[148:151], v15 offset:40960\n"
"buffer_load_dwordx4 v7, s[8:11], s63 offen offset:1024 lds\n"
"s_waitcnt lgkmcnt(6)\n"
"v_mfma_f32_16x16x32_bf16 v[72:75], a[72:75], v[156:159], v[72:75]\n"
"buffer_load_dwordx4 v8, s[8:11], s63 offen offset:2048 lds\n"
"v_mfma_f32_16x16x32_bf16 v[76:79], a[68:71], v[152:155], v[76:79]\n"
"ds_read_b128 v[152:155], v14 offset:43008\n"
"v_mfma_f32_16x16x32_bf16 v[76:79], a[76:79], v[156:159], v[76:79]\n"
"v_mfma_f32_16x16x32_bf16 v[24:27], v[32:35], a[64:67], v[24:27]\n"
"v_mfma_f32_16x16x32_bf16 v[24:27], v[32:35], a[72:75], v[24:27]\n"
"s_waitcnt lgkmcnt(4)\n"
"v_mfma_f32_16x16x32_bf16 v[80:83], a[64:67], v[160:163], v[80:83]\n"
"ds_read_b128 v[156:159], v15 offset:43008\n"
"buffer_load_dwordx4 v9, s[8:11], s63 offen offset:3072 lds\n"
"v_mfma_f32_16x16x32_bf16 v[80:83], a[72:75], v[164:167], v[80:83]\n"
"s_add_u32 m0, s75, 49152\n"
"s_mul_i32 s63, s73, 1792\n"
"v_mfma_f32_16x16x32_bf16 v[84:87], a[68:71], v[160:163], v[84:87]\n"
"ds_read_b128 v[160:163], v14 offset:45056\n"
"v_mfma_f32_16x16x32_bf16 v[84:87], a[76:79], v[164:167], v[84:87]\n"
"buffer_load_dwordx4 v10, s[12:15], s63 offen offset:0 lds\n"
"v_mfma_f32_16x16x32_bf16 v[88:91], a[64:67], v[168:171], v[88:91]\n"
"ds_read_b128 v[164:167], v15 offset:45056\n"
"s_waitcnt lgkmcnt(4)\n"
"v_mfma_f32_16x16x32_bf16 v[88:91], a[72:75], v[172:175], v[88:91]\n"
"v_mfma_f32_16x16x32_bf16 v[92:95], a[68:71], v[168:171], v[92:95]\n"
"ds_read_b128 v[168:171], v14 offset:47104\n"
"v_mfma_f32_16x16x32_bf16 v[92:95], a[76:79], v[172:175], v[92:95]\n"
"buffer_load_dwordx4 v11, s[12:15], s63 offen offset:1024 lds\n"
"v_mfma_f32_16x16x32_bf16 v[28:31], v[32:35], a[68:71], v[28:31]\n"
"v_mfma_f32_16x16x32_bf16 v[28:31], v[32:35], a[76:79], v[28:31]\n"
"v_mfma_f32_16x16x32_bf16 v[96:99], a[64:67], v[144:147], v[96:99]\n"
"ds_read_b128 v[172:175], v15 offset:47104\n"
"v_mfma_f32_16x16x32_bf16 v[96:99], a[72:75], v[148:151], v[96:99]\n"
"buffer_load_dwordx4 v12, s[12:15], s63 offen offset:2048 lds\n"
"v_mfma_f32_16x16x32_bf16 v[100:103], a[68:71], v[144:147], v[100:103]\n"
"v_mfma_f32_16x16x32_bf16 v[100:103], a[76:79], v[148:151], v[100:103]\n"
"s_waitcnt lgkmcnt(2)\n"
"v_mfma_f32_16x16x32_bf16 v[104:107], a[64:67], v[152:155], v[104:107]\n"
"v_mfma_f32_16x16x32_bf16 v[104:107], a[72:75], v[156:159], v[104:107]\n"
"buffer_load_dwordx4 v13, s[12:15], s63 offen offset:3072 lds\n"
"v_mfma_f32_16x16x32_bf16 v[108:111], a[68:71], v[152:155], v[108:111]\n"
"s_waitcnt vmcnt(12) lgkmcnt(0)\n"
"s_barrier\n"
"v_mfma_f32_16x16x32_bf16 v[108:111], a[76:79], v[156:159], v[108:111]\n"
"ds_read_b64_tr_b16 a[80:81], v5 offset:16384\n"
"ds_read_b64_tr_b16 a[82:83], v5 offset:16896\n"
"v_mfma_f32_16x16x32_bf16 v[112:115], a[64:67], v[160:163], v[112:115]\n"
"ds_read_b128 v[144:147], v14 offset:49152\n"
"ds_read_b64_tr_b16 a[88:89], v5 offset:24576\n"
"buffer_load_dwordx4 v[40:43], v236, s[20:23], 0 offen sc1\n"
"v_mfma_f32_16x16x32_bf16 v[112:115], a[72:75], v[164:167], v[112:115]\n"
"ds_read_b64_tr_b16 a[90:91], v5 offset:25088\n"
"ds_read_b128 v[148:151], v15 offset:49152\n"
"v_mfma_f32_16x16x32_bf16 v[116:119], a[68:71], v[160:163], v[116:119]\n"
"ds_read_b64_tr_b16 a[84:85], v5 offset:20480\n"
"ds_read_b64_tr_b16 a[86:87], v5 offset:20992\n"
"v_mfma_f32_16x16x32_bf16 v[116:119], a[76:79], v[164:167], v[116:119]\n"
"ds_read_b64_tr_b16 a[92:93], v5 offset:28672\n"
"ds_read_b64_tr_b16 a[94:95], v5 offset:29184\n"
"v_mfma_f32_16x16x32_bf16 v[120:123], a[64:67], v[168:171], v[120:123]\n"
"ds_read_b128 v[152:155], v14 offset:51200\n"
"ds_read_b128 v[156:159], v15 offset:51200\n"
"buffer_load_dwordx4 v[44:47], v237, s[20:23], 0 offen sc1\n"
"v_mfma_f32_16x16x32_bf16 v[120:123], a[72:75], v[172:175], v[120:123]\n"
"ds_read_b128 v[160:163], v14 offset:53248\n"
"s_add_u32 m0, s75, 81920\n"
"v_mfma_f32_16x16x32_bf16 v[124:127], a[68:71], v[168:171], v[124:127]\n"
"ds_read_b128 v[164:167], v15 offset:53248\n"
"s_add_u32 s63, s73, s74\n"
"v_mfma_f32_16x16x32_bf16 v[124:127], a[76:79], v[172:175], v[124:127]\n"
"ds_read_b128 v[168:171], v14 offset:55296\n"
"s_waitcnt lgkmcnt(12)\n"
"v_mfma_f32_16x16x32_bf16 v[64:67], a[80:83], v[144:147], v[64:67]\n"
"ds_read_b128 v[172:175], v15 offset:55296\n"
"s_waitcnt lgkmcnt(10)\n"
"v_mfma_f32_16x16x32_bf16 v[64:67], a[88:91], v[148:151], v[64:67]\n"
"buffer_load_dwordx4 v6, s[8:11], s63 offen offset:0 lds\n"
"s_waitcnt lgkmcnt(8)\n"
"v_mfma_f32_16x16x32_bf16 v[68:71], a[84:87], v[144:147], v[68:71]\n"
"ds_read_b128 v[144:147], v14 offset:57344\n"
"s_waitcnt lgkmcnt(7)\n"
"v_mfma_f32_16x16x32_bf16 v[68:71], a[92:95], v[148:151], v[68:71]\n"
"s_waitcnt lgkmcnt(6)\n"
"v_mfma_f32_16x16x32_bf16 v[72:75], a[80:83], v[152:155], v[72:75]\n"
"ds_read_b128 v[148:151], v15 offset:57344\n"
"buffer_load_dwordx4 v7, s[8:11], s63 offen offset:1024 lds\n"
"s_waitcnt lgkmcnt(6)\n"
"v_mfma_f32_16x16x32_bf16 v[72:75], a[88:91], v[156:159], v[72:75]\n"
"v_mfma_f32_16x16x32_bf16 v[76:79], a[84:87], v[152:155], v[76:79]\n"
"ds_read_b128 v[152:155], v14 offset:59392\n"
"v_mfma_f32_16x16x32_bf16 v[76:79], a[92:95], v[156:159], v[76:79]\n"
"buffer_load_dwordx4 v8, s[8:11], s63 offen offset:2048 lds\n"
"v_mfma_f32_16x16x32_bf16 v[24:27], v[32:35], a[80:83], v[24:27]\n"
"v_mfma_f32_16x16x32_bf16 v[24:27], v[32:35], a[88:91], v[24:27]\n"
"s_waitcnt lgkmcnt(3)\n"
"v_mfma_f32_16x16x32_bf16 v[80:83], a[80:83], v[160:163], v[80:83]\n"
"ds_read_b128 v[156:159], v15 offset:59392\n"
"v_mfma_f32_16x16x32_bf16 v[80:83], a[88:91], v[164:167], v[80:83]\n"
"buffer_load_dwordx4 v9, s[8:11], s63 offen offset:3072 lds\n"
"v_mfma_f32_16x16x32_bf16 v[84:87], a[84:87], v[160:163], v[84:87]\n"
"ds_read_b128 v[160:163], v14 offset:61440\n"
"s_add_u32 m0, s75, 98304\n"
"v_mfma_f32_16x16x32_bf16 v[84:87], a[92:95], v[164:167], v[84:87]\n"
"s_add_u32 s63, s44, s46\n"
"v_mfma_f32_16x16x32_bf16 v[88:91], a[80:83], v[168:171], v[88:91]\n"
"ds_read_b128 v[164:167], v15 offset:61440\n"
"v_mfma_f32_16x16x32_bf16 v[88:91], a[88:91], v[172:175], v[88:91]\n"
"buffer_load_dwordx4 v6, s[8:11], s63 offen offset:0 lds\n"
"v_mfma_f32_16x16x32_bf16 v[92:95], a[84:87], v[168:171], v[92:95]\n"
"ds_read_b128 v[168:171], v14 offset:63488\n"
"v_mfma_f32_16x16x32_bf16 v[92:95], a[92:95], v[172:175], v[92:95]\n"
"v_mfma_f32_16x16x32_bf16 v[28:31], v[32:35], a[84:87], v[28:31]\n"
"v_mfma_f32_16x16x32_bf16 v[28:31], v[32:35], a[92:95], v[28:31]\n"
"buffer_load_dwordx4 v7, s[8:11], s63 offen offset:1024 lds\n"
"s_waitcnt lgkmcnt(3)\n"
"v_mfma_f32_16x16x32_bf16 v[96:99], a[80:83], v[144:147], v[96:99]\n"
"ds_read_b128 v[172:175], v15 offset:63488\n"
"v_mfma_f32_16x16x32_bf16 v[96:99], a[88:91], v[148:151], v[96:99]\n"
"v_mfma_f32_16x16x32_bf16 v[100:103], a[84:87], v[144:147], v[100:103]\n"
"v_mfma_f32_16x16x32_bf16 v[100:103], a[92:95], v[148:151], v[100:103]\n"
"buffer_load_dwordx4 v8, s[8:11], s63 offen offset:2048 lds\n"
"v_mfma_f32_16x16x32_bf16 v[104:107], a[80:83], v[152:155], v[104:107]\n"
"v_mfma_f32_16x16x32_bf16 v[104:107], a[88:91], v[156:159], v[104:107]\n"
"v_mfma_f32_16x16x32_bf16 v[108:111], a[84:87], v[152:155], v[108:111]\n"
"s_waitcnt vmcnt(13) lgkmcnt(0)\n"
"s_barrier\n"
"buffer_load_dwordx4 v9, s[8:11], s63 offen offset:3072 lds\n"
"v_mfma_f32_16x16x32_bf16 v[108:111], a[92:95], v[156:159], v[108:111]\n"
"ds_read_b64_tr_b16 a[96:97], v5 offset:32768\n"
"ds_read_b64_tr_b16 a[98:99], v5 offset:33280\n"
"v_mfma_f32_16x16x32_bf16 v[112:115], a[80:83], v[160:163], v[112:115]\n"
"ds_read_b128 v[144:147], v14 offset:0\n"
"ds_read_b64_tr_b16 a[104:105], v5 offset:40960\n"
"v_mfma_f32_16x16x32_bf16 v[112:115], a[88:91], v[164:167], v[112:115]\n"
"ds_read_b64_tr_b16 a[106:107], v5 offset:41472\n"
"ds_read_b128 v[148:151], v15 offset:0\n"
"buffer_load_dwordx4 v[48:51], v238, s[20:23], 0 offen sc1\n"
"v_mfma_f32_16x16x32_bf16 v[116:119], a[84:87], v[160:163], v[116:119]\n"
"ds_read_b64_tr_b16 a[100:101], v5 offset:36864\n"
"ds_read_b64_tr_b16 a[102:103], v5 offset:37376\n"
"v_mfma_f32_16x16x32_bf16 v[116:119], a[92:95], v[164:167], v[116:119]\n"
"ds_read_b64_tr_b16 a[108:109], v5 offset:45056\n"
"ds_read_b64_tr_b16 a[110:111], v5 offset:45568\n"
"v_mfma_f32_16x16x32_bf16 v[120:123], a[80:83], v[168:171], v[120:123]\n"
"ds_read_b128 v[152:155], v14 offset:2048\n"
"ds_read_b128 v[156:159], v15 offset:2048\n"
"v_mfma_f32_16x16x32_bf16 v[120:123], a[88:91], v[172:175], v[120:123]\n"
"s_add_u32 m0, s75, 131072\n"
"ds_read_b128 v[160:163], v14 offset:4096\n"
"buffer_load_dwordx4 v[52:55], v239, s[20:23], 0 offen sc1\n"
"v_mfma_f32_16x16x32_bf16 v[124:127], a[84:87], v[168:171], v[124:127]\n"
"ds_read_b128 v[164:167], v15 offset:4096\n"
"s_lshl_b32 s63, s88, 6\n"
"v_mfma_f32_16x16x32_bf16 v[124:127], a[92:95], v[172:175], v[124:127]\n"
"ds_read_b128 v[168:171], v14 offset:6144\n"
"s_waitcnt lgkmcnt(12)\n"
"v_mfma_f32_16x16x32_bf16 v[64:67], a[96:99], v[144:147], v[64:67]\n"
"ds_read_b128 v[172:175], v15 offset:6144\n"
"s_waitcnt lgkmcnt(10)\n"
"v_mfma_f32_16x16x32_bf16 v[64:67], a[104:107], v[148:151], v[64:67]\n"
"buffer_load_dwordx4 v18, s[24:27], s63 offen offset:0 lds\n"
"s_waitcnt lgkmcnt(8)\n"
"v_mfma_f32_16x16x32_bf16 v[68:71], a[100:103], v[144:147], v[68:71]\n"
"ds_read_b128 v[144:147], v14 offset:8192\n"
"s_waitcnt lgkmcnt(7)\n"
"v_mfma_f32_16x16x32_bf16 v[68:71], a[108:111], v[148:151], v[68:71]\n"
"s_waitcnt lgkmcnt(6)\n"
"v_mfma_f32_16x16x32_bf16 v[72:75], a[96:99], v[152:155], v[72:75]\n"
"ds_read_b128 v[148:151], v15 offset:8192\n"
"buffer_load_dwordx4 v19, s[24:27], s63 offen offset:1024 lds\n"
"s_waitcnt lgkmcnt(6)\n"
"v_mfma_f32_16x16x32_bf16 v[72:75], a[104:107], v[156:159], v[72:75]\n"
"v_mfma_f32_16x16x32_bf16 v[76:79], a[100:103], v[152:155], v[76:79]\n"
"ds_read_b128 v[152:155], v14 offset:10240\n"
"v_mfma_f32_16x16x32_bf16 v[76:79], a[108:111], v[156:159], v[76:79]\n"
"v_mfma_f32_16x16x32_bf16 v[24:27], v[32:35], a[96:99], v[24:27]\n"
"buffer_load_dwordx4 v20, s[24:27], s63 offen offset:2048 lds\n"
"v_mfma_f32_16x16x32_bf16 v[24:27], v[32:35], a[104:107], v[24:27]\n"
"s_waitcnt lgkmcnt(3)\n"
"v_mfma_f32_16x16x32_bf16 v[80:83], a[96:99], v[160:163], v[80:83]\n"
"ds_read_b128 v[156:159], v15 offset:10240\n"
"v_mfma_f32_16x16x32_bf16 v[80:83], a[104:107], v[164:167], v[80:83]\n"
"v_mfma_f32_16x16x32_bf16 v[84:87], a[100:103], v[160:163], v[84:87]\n"
"ds_read_b128 v[160:163], v14 offset:12288\n"
"buffer_load_dwordx4 v21, s[24:27], s63 offen offset:3072 lds\n"
"v_mfma_f32_16x16x32_bf16 v[84:87], a[108:111], v[164:167], v[84:87]\n"
"s_add_u32 m0, s75, 114688\n"
"s_add_u32 s63, s44, s47\n"
"v_mfma_f32_16x16x32_bf16 v[88:91], a[96:99], v[168:171], v[88:91]\n"
"ds_read_b128 v[164:167], v15 offset:12288\n"
"v_mfma_f32_16x16x32_bf16 v[88:91], a[104:107], v[172:175], v[88:91]\n"
"v_mfma_f32_16x16x32_bf16 v[92:95], a[100:103], v[168:171], v[92:95]\n"
"ds_read_b128 v[168:171], v14 offset:14336\n"
"buffer_load_dwordx4 v6, s[8:11], s63 offen offset:0 lds\n"
"v_mfma_f32_16x16x32_bf16 v[92:95], a[108:111], v[172:175], v[92:95]\n"
"v_mfma_f32_16x16x32_bf16 v[28:31], v[32:35], a[100:103], v[28:31]\n"
"v_mfma_f32_16x16x32_bf16 v[28:31], v[32:35], a[108:111], v[28:31]\n"
"s_waitcnt lgkmcnt(3)\n"
"v_mfma_f32_16x16x32_bf16 v[96:99], a[96:99], v[144:147], v[96:99]\n"
"ds_read_b128 v[172:175], v15 offset:14336\n"
"buffer_load_dwordx4 v7, s[8:11], s63 offen offset:1024 lds\n"
"v_mfma_f32_16x16x32_bf16 v[96:99], a[104:107], v[148:151], v[96:99]\n"
"v_mfma_f32_16x16x32_bf16 v[100:103], a[100:103], v[144:147], v[100:103]\n"
"v_mfma_f32_16x16x32_bf16 v[100:103], a[108:111], v[148:151], v[100:103]\n"
"buffer_load_dwordx4 v8, s[8:11], s63 offen offset:2048 lds\n"
"v_mfma_f32_16x16x32_bf16 v[104:107], a[96:99], v[152:155], v[104:107]\n"
"v_mfma_f32_16x16x32_bf16 v[104:107], a[104:107], v[156:159], v[104:107]\n"
"v_mfma_f32_16x16x32_bf16 v[108:111], a[100:103], v[152:155], v[108:111]\n"
"s_waitcnt vmcnt(13) lgkmcnt(0)\n"
"s_barrier\n"
"v_mfma_f32_16x16x32_bf16 v[108:111], a[108:111], v[156:159], v[108:111]\n"
"ds_read_b64_tr_b16 a[112:113], v5 offset:49152\n"
"ds_read_b64_tr_b16 a[114:115], v5 offset:49664\n"
"v_mfma_f32_16x16x32_bf16 v[112:115], a[96:99], v[160:163], v[112:115]\n"
"ds_read_b128 v[144:147], v14 offset:16384\n"
"ds_read_b64_tr_b16 a[120:121], v5 offset:57344\n"
"v_mfma_f32_16x16x32_bf16 v[112:115], a[104:107], v[164:167], v[112:115]\n"
"ds_read_b64_tr_b16 a[122:123], v5 offset:57856\n"
"ds_read_b128 v[148:151], v15 offset:16384\n"
"buffer_load_dwordx4 v9, s[8:11], s63 offen offset:3072 lds\n"
"v_mfma_f32_16x16x32_bf16 v[116:119], a[100:103], v[160:163], v[116:119]\n"
"ds_read_b64_tr_b16 a[116:117], v5 offset:53248\n"
"ds_read_b64_tr_b16 a[118:119], v5 offset:53760\n"
"v_mfma_f32_16x16x32_bf16 v[116:119], a[108:111], v[164:167], v[116:119]\n"
"ds_read_b64_tr_b16 a[124:125], v5 offset:61440\n"
"ds_read_b64_tr_b16 a[126:127], v5 offset:61952\n"
"v_mfma_f32_16x16x32_bf16 v[120:123], a[96:99], v[168:171], v[120:123]\n"
"ds_read_b128 v[152:155], v14 offset:18432\n"
"ds_read_b128 v[156:159], v15 offset:18432\n"
"v_mfma_f32_16x16x32_bf16 v[120:123], a[104:107], v[172:175], v[120:123]\n"
"s_add_u32 m0, s75, 147456\n"
"ds_read_b128 v[160:163], v14 offset:20480\n"
"s_lshl_b32 s63, s89, 6\n"
"v_mfma_f32_16x16x32_bf16 v[124:127], a[100:103], v[168:171], v[124:127]\n"
"ds_read_b128 v[164:167], v15 offset:20480\n"
"v_mfma_f32_16x16x32_bf16 v[124:127], a[108:111], v[172:175], v[124:127]\n"
"ds_read_b128 v[168:171], v14 offset:22528\n"
"buffer_load_dwordx4 v18, s[24:27], s63 offen offset:0 lds\n"
"s_waitcnt lgkmcnt(12)\n"
"v_mfma_f32_16x16x32_bf16 v[64:67], a[112:115], v[144:147], v[64:67]\n"
"ds_read_b128 v[172:175], v15 offset:22528\n"
"s_waitcnt lgkmcnt(10)\n"
"v_mfma_f32_16x16x32_bf16 v[64:67], a[120:123], v[148:151], v[64:67]\n"
"s_waitcnt lgkmcnt(8)\n"
"v_mfma_f32_16x16x32_bf16 v[68:71], a[116:119], v[144:147], v[68:71]\n"
"ds_read_b128 v[144:147], v14 offset:24576\n"
"buffer_load_dwordx4 v19, s[24:27], s63 offen offset:1024 lds\n"
"s_waitcnt lgkmcnt(7)\n"
"v_mfma_f32_16x16x32_bf16 v[68:71], a[124:127], v[148:151], v[68:71]\n"
"s_waitcnt lgkmcnt(6)\n"
"v_mfma_f32_16x16x32_bf16 v[72:75], a[112:115], v[152:155], v[72:75]\n"
"ds_read_b128 v[148:151], v15 offset:24576\n"
"s_waitcnt lgkmcnt(6)\n"
"v_mfma_f32_16x16x32_bf16 v[72:75], a[120:123], v[156:159], v[72:75]\n"
"buffer_load_dwordx4 v20, s[24:27], s63 offen offset:2048 lds\n"
"v_mfma_f32_16x16x32_bf16 v[76:79], a[116:119], v[152:155], v[76:79]\n"
"ds_read_b128 v[152:155], v14 offset:26624\n"
"v_mfma_f32_16x16x32_bf16 v[76:79], a[124:127], v[156:159], v[76:79]\n"
"buffer_load_dwordx4 v21, s[24:27], s63 offen offset:3072 lds\n"
"v_mfma_f32_16x16x32_bf16 v[24:27], v[32:35], a[112:115], v[24:27]\n"
"s_waitcnt vmcnt(12)\n"
"v_xor_b32 v22, v41, v2\n"
"v_bitop3_b32 v22, v22, v43, v2 bitop3:0xf6\n"
"v_mfma_f32_16x16x32_bf16 v[24:27], v[32:35], a[120:123], v[24:27]\n"
"v_bitop3_b32 v22, v22, v45, v2 bitop3:0xf6\n"
"v_bitop3_b32 v22, v22, v47, v2 bitop3:0xf6\n"
"v_bitop3_b32 v22, v22, v49, v2 bitop3:0xf6\n"
"s_waitcnt lgkmcnt(3)\n"
"v_mfma_f32_16x16x32_bf16 v[80:83], a[112:115], v[160:163], v[80:83]\n"
"ds_read_b128 v[156:159], v15 offset:26624\n"
"s_add_u32 m0, s75, 65536\n"
"v_bitop3_b32 v22, v22, v51, v2 bitop3:0xf6\n"
"v_mfma_f32_16x16x32_bf16 v[80:83], a[120:123], v[164:167], v[80:83]\n"
"s_add_u32 s63, s44, s48\n"
"v_bitop3_b32 v22, v22, v53, v2 bitop3:0xf6\n"
"v_bitop3_b32 v22, v22, v55, v2 bitop3:0xf6\n"
"v_mfma_f32_16x16x32_bf16 v[84:87], a[116:119], v[160:163], v[84:87]\n"
"ds_read_b128 v[160:163], v14 offset:28672\n"
"v_cmp_ne_u32 vcc, 0, v22\n"
"s_cbranch_vccnz LslowS_m_%=\n"
"LokS_m_%=:\n"
"v_mfma_f32_16x16x32_bf16 v[84:87], a[124:127], v[164:167], v[84:87]\n"
"buffer_load_dwordx4 v6, s[8:11], s63 offen offset:0 lds\n"
"v_perm_b32 v41, v40, v40, s58\n"
"v_mfma_f32_16x16x32_bf16 v[88:91], a[112:115], v[168:171], v[88:91]\n"
"ds_read_b128 v[164:167], v15 offset:28672\n"
"v_perm_b32 v43, v42, v42, s58\n"
"v_perm_b32 v45, v44, v44, s58\n"
"v_mfma_f32_16x16x32_bf16 v[88:91], a[120:123], v[172:175], v[88:91]\n"
"v_perm_b32 v47, v46, v46, s58\n"
"v_perm_b32 v49, v48, v48, s58\n"
"v_perm_b32 v51, v50, v50, s58\n"
"v_mfma_f32_16x16x32_bf16 v[92:95], a[116:119], v[168:171], v[92:95]\n"
"ds_read_b128 v[168:171], v14 offset:30720\n"
"v_perm_b32 v53, v52, v52, s58\n"
"v_perm_b32 v55, v54, v54, s58\n"
"v_mfma_f32_16x16x32_bf16 v[92:95], a[124:127], v[172:175], v[92:95]\n"
"buffer_load_dwordx4 v7, s[8:11], s63 offen offset:1024 lds\n"
"v_mfma_f32_16x16x32_bf16 v[28:31], v[32:35], a[116:119], v[28:31]\n"
"v_mfma_f32_16x16x32_bf16 v[28:31], v[32:35], a[124:127], v[28:31]\n"
"v_and_b32 v25, 0xffff0000, v24\n"
"s_waitcnt lgkmcnt(3)\n"
"v_mfma_f32_16x16x32_bf16 v[96:99], a[112:115], v[144:147], v[96:99]\n"
"ds_read_b128 v[172:175], v15 offset:30720\n"
"v_sub_f32 v26, v24, v25\n"
"v_mfma_f32_16x16x32_bf16 v[96:99], a[120:123], v[148:151], v[96:99]\n"
"v_and_b32 v27, 0xffff0000, v26\n"
"v_sub_f32 v26, v26, v27\n"
"v_lshrrev_b32 v24, 16, v24\n"
"v_mfma_f32_16x16x32_bf16 v[100:103], a[116:119], v[144:147], v[100:103]\n"
"buffer_load_dwordx4 v8, s[8:11], s63 offen offset:2048 lds\n"
"v_perm_b32 v25, v26, v27, s59\n"
"v_mfma_f32_16x16x32_bf16 v[100:103], a[124:127], v[148:151], v[100:103]\n"
"v_and_b32 v29, 0xffff0000, v28\n"
"v_sub_f32 v30, v28, v29\n"
"v_and_b32 v31, 0xffff0000, v30\n"
"v_mfma_f32_16x16x32_bf16 v[104:107], a[112:115], v[152:155], v[104:107]\n"
"v_sub_f32 v30, v30, v31\n"
"v_lshrrev_b32 v28, 16, v28\n"
"v_perm_b32 v29, v30, v31, s59\n"
"v_mfma_f32_16x16x32_bf16 v[104:107], a[120:123], v[156:159], v[104:107]\n"
"v_lshrrev_b32 v22, 4, v0\n"
"v_cmp_eq_u32 s[66:67], 0, v22\n"
"s_nop 1\n"
"v_mfma_f32_16x16x32_bf16 v[108:111], a[116:119], v[152:155], v[108:111]\n"
"s_waitcnt lgkmcnt(0)\n"
"s_barrier\n"
"v_mfma_f32_16x16x32_bf16 v[108:111], a[124:127], v[156:159], v[108:111]\n"
"ds_read_b128 v[144:147], v14 offset:32768\n"
"ds_read_b128 v[148:151], v15 offset:32768\n"
"v_cndmask_b32 v23, 0, v24, s[66:67]\n"
"v_mfma_f32_16x16x32_bf16 v[112:115], a[112:115], v[160:163], v[112:115]\n"
"ds_read_b128 v[152:155], v14 offset:36864\n"
"v_accvgpr_write_b32 a240, v23\n"
"v_cndmask_b32 v23, 0, v25, s[66:67]\n"
"v_mfma_f32_16x16x32_bf16 v[112:115], a[120:123], v[164:167], v[112:115]\n"
"ds_read_b128 v[156:159], v15 offset:36864\n"
"buffer_load_dwordx4 v9, s[8:11], s63 offen offset:3072 lds\n"
"v_mfma_f32_16x16x32_bf16 v[116:119], a[116:119], v[160:163], v[116:119]\n"
"ds_read_b128 v[160:163], v14 offset:40960\n"
"s_add_u32 m0, s95, 0\n"
"v_accvgpr_write_b32 a241, v23\n"
"v_mfma_f32_16x16x32_bf16 v[116:119], a[124:127], v[164:167], v[116:119]\n"
"ds_read_b128 v[164:167], v15 offset:40960\n"
"s_add_u32 s63, s96, 0\n"
"v_cndmask_b32 v23, 0, v28, s[66:67]\n"
"v_mfma_f32_16x16x32_bf16 v[120:123], a[112:115], v[168:171], v[120:123]\n"
"buffer_load_dwordx4 v1, s[28:31], s63 offen offset:0 lds\n"
"v_accvgpr_write_b32 a244, v23\n"
"v_mfma_f32_16x16x32_bf16 v[120:123], a[120:123], v[172:175], v[120:123]\n"
"v_cndmask_b32 v23, 0, v29, s[66:67]\n"
"v_accvgpr_write_b32 a245, v23\n"
"v_cmp_eq_u32 s[66:67], 1, v22\n"
"v_mfma_f32_16x16x32_bf16 v[124:127], a[116:119], v[168:171], v[124:127]\n"
"ds_read_b128 v[168:171], v14 offset:45056\n"
"s_nop 1\n"
"v_cndmask_b32 v23, 0, v24, s[66:67]\n"
"v_mfma_f32_16x16x32_bf16 v[124:127], a[124:127], v[172:175], v[124:127]\n"
"ds_read_b128 v[172:175], v15 offset:45056\n"
"v_accvgpr_write_b32 a248, v23\n"
"v_cndmask_b32 v23, 0, v25, s[66:67]\n"
"v_accvgpr_write_b32 a249, v23\n"
"v_cndmask_b32 v23, 0, v28, s[66:67]\n"
"v_accvgpr_write_b32 a252, v23\n"
"v_cndmask_b32 v23, 0, v29, s[66:67]\n"
"v_accvgpr_write_b32 a253, v23\n"
"v_mfma_f32_16x16x32_bf16 v[64:67], a[240:243], v[40:43], v[64:67]\n"
"s_waitcnt vmcnt(13)\n"
"ds_read_b64_tr_b16 a[128:129], v232 offset:32768\n"
"v_mfma_f32_16x16x32_bf16 v[68:71], a[244:247], v[40:43], v[68:71]\n"
"ds_read_b64_tr_b16 a[130:131], v232 offset:33280\n"
"ds_read_b64_tr_b16 a[132:133], v232 offset:32776\n"
"v_mfma_f32_16x16x32_bf16 v[72:75], a[248:251], v[40:43], v[72:75]\n"
"v_mfma_f32_16x16x32_bf16 v[76:79], a[252:255], v[40:43], v[76:79]\n"
"v_mfma_f32_16x16x32_bf16 v[80:83], a[240:243], v[42:45], v[80:83]\n"
"v_max_f32 v64, 0, v64\n"
"v_mfma_f32_16x16x32_bf16 v[84:87], a[244:247], v[42:45], v[84:87]\n"
"v_mfma_f32_16x16x32_bf16 v[88:91], a[248:251], v[42:45], v[88:91]\n"
"v_mul_f32 v64, v64, v68\n"
"v_max_f32 v65, 0, v65\n"
"v_mul_f32 v65, v65, v69\n"
"v_mfma_f32_16x16x32_bf16 v[92:95], a[252:255], v[42:45], v[92:95]\n"
"v_max_f32 v66, 0, v66\n"
"v_mul_f32 v66, v66, v70\n"
"v_max_f32 v67, 0, v67\n"
"v_mfma_f32_16x16x32_bf16 v[96:99], a[240:243], v[44:47], v[96:99]\n"
"v_mul_f32 v67, v67, v71\n"
"v_cvt_pk_bf16_f32 v68, v64, v65\n"
"v_cvt_pk_bf16_f32 v69, v66, v67\n"
"v_mfma_f32_16x16x32_bf16 v[100:103], a[244:247], v[44:47], v[100:103]\n"
"v_mfma_f32_16x16x32_bf16 v[104:107], a[248:251], v[44:47], v[104:107]\n"
"v_mfma_f32_16x16x32_bf16 v[108:111], a[252:255], v[44:47], v[108:111]\n"
"v_mfma_f32_16x16x32_bf16 v[112:115], a[240:243], v[46:49], v[112:115]\n"
"v_mfma_f32_16x16x32_bf16 v[116:119], a[244:247], v[46:49], v[116:119]\n"
"v_mfma_f32_16x16x32_bf16 v[120:123], a[248:251], v[46:49], v[120:123]\n"
"v_max_f32 v72, 0, v72\n"
"v_mul_f32 v72, v72, v76\n"
"v_mfma_f32_16x16x32_bf16 v[124:127], a[252:255], v[46:49], v[124:127]\n"
"v_max_f32 v73, 0, v73\n"
"v_mul_f32 v73, v73, v77\n"
"v_mfma_f32_16x16x32_bf16 v[128:131], a[240:243], v[48:51], 0\n"
"v_max_f32 v74, 0, v74\n"
"v_mul_f32 v74, v74, v78\n"
"v_max_f32 v75, 0, v75\n"
"v_mfma_f32_16x16x32_bf16 v[132:135], a[244:247], v[48:51], 0\n"
"v_mul_f32 v75, v75, v79\n"
"v_cvt_pk_bf16_f32 v70, v72, v73\n"
"v_cvt_pk_bf16_f32 v71, v74, v75\n"
"s_waitcnt lgkmcnt(0)\n"
"v_mfma_f32_16x16x32_bf16 v[132:135], a[4:7], v[144:147], v[132:135]\n"
"buffer_load_dwordx4 v1, s[28:31], s63 offen offset:1024 lds\n"
"v_permlane16_swap_b32 v68, v70\n"
"v_mfma_f32_16x16x32_bf16 v[132:135], a[12:15], v[148:151], v[132:135]\n"
"v_permlane16_swap_b32 v69, v71\n"
"v_mfma_f32_16x16x32_bf16 v[132:135], a[20:23], v[152:155], v[132:135]\n"
"ds_read_b64_tr_b16 a[134:135], v232 offset:33288\n"
"ds_read_b64_tr_b16 a[136:137], v233 offset:32768\n"
"v_mfma_f32_16x16x32_bf16 v[132:135], a[28:31], v[156:159], v[132:135]\n"
"v_mfma_f32_16x16x32_bf16 v[128:131], a[0:3], v[144:147], v[128:131]\n"
"ds_read_b128 v[144:147], v14 offset:34816\n"
"buffer_store_dwordx4 v[68:71], v1, s[40:43], s50 offen sc1\n"
"v_mfma_f32_16x16x32_bf16 v[128:131], a[8:11], v[148:151], v[128:131]\n"
"ds_read_b128 v[148:151], v15 offset:34816\n"
"s_add_u32 s64, s50, 114688\n"
"v_mfma_f32_16x16x32_bf16 v[128:131], a[16:19], v[152:155], v[128:131]\n"
"ds_read_b128 v[152:155], v14 offset:38912\n"
"v_max_f32 v80, 0, v80\n"
"v_mfma_f32_16x16x32_bf16 v[128:131], a[24:27], v[156:159], v[128:131]\n"
"ds_read_b128 v[156:159], v15 offset:38912\n"
"buffer_load_dwordx4 v1, s[28:31], s63 offen offset:2048 lds\n"
"v_mfma_f32_16x16x32_bf16 v[128:131], a[32:35], v[160:163], v[128:131]\n"
"v_mul_f32 v80, v80, v84\n"
"v_max_f32 v81, 0, v81\n"
"v_mul_f32 v81, v81, v85\n"
"v_mfma_f32_16x16x32_bf16 v[128:131], a[40:43], v[164:167], v[128:131]\n"
"buffer_load_dwordx4 v1, s[28:31], s63 offen offset:3072 lds\n"
"v_max_f32 v82, 0, v82\n"
"v_mfma_f32_16x16x32_bf16 v[128:131], a[48:51], v[168:171], v[128:131]\n"
"v_mul_f32 v82, v82, v86\n"
"v_max_f32 v83, 0, v83\n"
"v_mul_f32 v83, v83, v87\n"
"v_mfma_f32_16x16x32_bf16 v[128:131], a[56:59], v[172:175], v[128:131]\n"
"v_cvt_pk_bf16_f32 v84, v80, v81\n"
"v_cvt_pk_bf16_f32 v85, v82, v83\n"
"v_mfma_f32_16x16x32_bf16 v[132:135], a[36:39], v[160:163], v[132:135]\n"
"ds_read_b128 v[160:163], v14 offset:43008\n"
"ds_read_b64_tr_b16 a[138:139], v233 offset:33280\n"
"v_mfma_f32_16x16x32_bf16 v[132:135], a[44:47], v[164:167], v[132:135]\n"
"ds_read_b128 v[164:167], v15 offset:43008\n"
"ds_read_b64_tr_b16 a[140:141], v233 offset:32776\n"
"v_mfma_f32_16x16x32_bf16 v[132:135], a[52:55], v[168:171], v[132:135]\n"
"ds_read_b128 v[168:171], v14 offset:47104\n"
"s_add_u32 m0, s75, 81920\n"
"v_mfma_f32_16x16x32_bf16 v[132:135], a[60:63], v[172:175], v[132:135]\n"
"ds_read_b128 v[172:175], v15 offset:47104\n"
"s_add_u32 s63, s44, s49\n"
"v_mfma_f32_16x16x32_bf16 v[136:139], a[248:251], v[48:51], 0\n"
"v_max_f32 v88, 0, v88\n"
"v_mul_f32 v88, v88, v92\n"
"ds_read_b64_tr_b16 a[142:143], v233 offset:33288\n"
"v_mfma_f32_16x16x32_bf16 v[140:143], a[252:255], v[48:51], 0\n"
"v_max_f32 v89, 0, v89\n"
"s_waitcnt lgkmcnt(7)\n"
"v_mfma_f32_16x16x32_bf16 v[140:143], a[4:7], v[144:147], v[140:143]\n"
"buffer_load_dwordx4 v6, s[8:11], s63 offen offset:0 lds\n"
"v_mul_f32 v89, v89, v93\n"
"v_mfma_f32_16x16x32_bf16 v[140:143], a[12:15], v[148:151], v[140:143]\n"
"v_max_f32 v90, 0, v90\n"
"v_mul_f32 v90, v90, v94\n"
"v_max_f32 v91, 0, v91\n"
"v_mfma_f32_16x16x32_bf16 v[140:143], a[20:23], v[152:155], v[140:143]\n"
"v_mul_f32 v91, v91, v95\n"
"v_cvt_pk_bf16_f32 v86, v88, v89\n"
"v_cvt_pk_bf16_f32 v87, v90, v91\n"
"v_mfma_f32_16x16x32_bf16 v[140:143], a[28:31], v[156:159], v[140:143]\n"
"v_permlane16_swap_b32 v84, v86\n"
"v_permlane16_swap_b32 v85, v87\n"
"v_mfma_f32_16x16x32_bf16 v[136:139], a[0:3], v[144:147], v[136:139]\n"
"buffer_store_dwordx4 v[84:87], v1, s[40:43], s64 offen sc1\n"
"v_mfma_f32_16x16x32_bf16 v[136:139], a[8:11], v[148:151], v[136:139]\n"
"v_mfma_f32_16x16x32_bf16 v[136:139], a[16:19], v[152:155], v[136:139]\n"
"s_waitcnt vmcnt(15) lgkmcnt(0)\n"
"s_barrier\n"
"v_mfma_f32_16x16x32_bf16 v[136:139], a[24:27], v[156:159], v[136:139]\n"
"ds_read_b128 v[144:147], v14 offset:49152\n"
"ds_read_b128 v[148:151], v15 offset:49152\n"
"v_mfma_f32_16x16x32_bf16 v[136:139], a[32:35], v[160:163], v[136:139]\n"
"ds_read_b128 v[152:155], v14 offset:53248\n"
"v_max_f32 v96, 0, v96\n"
"v_mfma_f32_16x16x32_bf16 v[136:139], a[40:43], v[164:167], v[136:139]\n"
"ds_read_b128 v[156:159], v15 offset:53248\n"
"buffer_load_dwordx4 v7, s[8:11], s63 offen offset:1024 lds\n"
"v_mfma_f32_16x16x32_bf16 v[136:139], a[48:51], v[168:171], v[136:139]\n"
"v_mul_f32 v96, v96, v100\n"
"v_max_f32 v97, 0, v97\n"
"v_mul_f32 v97, v97, v101\n"
"v_mfma_f32_16x16x32_bf16 v[136:139], a[56:59], v[172:175], v[136:139]\n"
"v_max_f32 v98, 0, v98\n"
"v_mul_f32 v98, v98, v102\n"
"v_max_f32 v99, 0, v99\n"
"v_mfma_f32_16x16x32_bf16 v[140:143], a[36:39], v[160:163], v[140:143]\n"
"ds_read_b128 v[160:163], v14 offset:57344\n"
"v_mul_f32 v99, v99, v103\n"
"v_cvt_pk_bf16_f32 v100, v96, v97\n"
"v_mfma_f32_16x16x32_bf16 v[140:143], a[44:47], v[164:167], v[140:143]\n"
"ds_read_b128 v[164:167], v15 offset:57344\n"
"v_cvt_pk_bf16_f32 v101, v98, v99\n"
"v_mfma_f32_16x16x32_bf16 v[140:143], a[52:55], v[168:171], v[140:143]\n"
"ds_read_b128 v[168:171], v14 offset:61440\n"
"v_mfma_f32_16x16x32_bf16 v[140:143], a[60:63], v[172:175], v[140:143]\n"
"ds_read_b128 v[172:175], v15 offset:61440\n"
"buffer_load_dwordx4 v8, s[8:11], s63 offen offset:2048 lds\n"
"s_waitcnt lgkmcnt(7)\n"
"v_mfma_f32_16x16x32_bf16 v[132:135], a[68:71], v[144:147], v[132:135]\n"
"s_waitcnt vmcnt(13)\n"
"ds_read_b64_tr_b16 a[144:145], v232 offset:49152\n"
"s_waitcnt lgkmcnt(6)\n"
"v_mfma_f32_16x16x32_bf16 v[132:135], a[76:79], v[148:151], v[132:135]\n"
"ds_read_b64_tr_b16 a[146:147], v232 offset:49664\n"
"ds_read_b64_tr_b16 a[148:149], v232 offset:49160\n"
"v_mfma_f32_16x16x32_bf16 v[132:135], a[84:87], v[152:155], v[132:135]\n"
"v_max_f32 v104, 0, v104\n"
"s_waitcnt lgkmcnt(7)\n"
"v_mfma_f32_16x16x32_bf16 v[132:135], a[92:95], v[156:159], v[132:135]\n"
"buffer_load_dwordx4 v9, s[8:11], s63 offen offset:3072 lds\n"
"v_mfma_f32_16x16x32_bf16 v[128:131], a[64:67], v[144:147], v[128:131]\n"
"ds_read_b128 v[144:147], v14 offset:51200\n"
"v_mul_f32 v104, v104, v108\n"
"v_max_f32 v105, 0, v105\n"
"v_mfma_f32_16x16x32_bf16 v[128:131], a[72:75], v[148:151], v[128:131]\n"
"ds_read_b128 v[148:151], v15 offset:51200\n"
"v_mul_f32 v105, v105, v109\n"
"v_max_f32 v106, 0, v106\n"
"v_mfma_f32_16x16x32_bf16 v[128:131], a[80:83], v[152:155], v[128:131]\n"
"ds_read_b128 v[152:155], v14 offset:55296\n"
"v_mul_f32 v106, v106, v110\n"
"v_max_f32 v107, 0, v107\n"
"v_mfma_f32_16x16x32_bf16 v[128:131], a[88:91], v[156:159], v[128:131]\n"
"ds_read_b128 v[156:159], v15 offset:55296\n"
"v_mul_f32 v107, v107, v111\n"
"v_cvt_pk_bf16_f32 v102, v104, v105\n"
"s_waitcnt lgkmcnt(6)\n"
"v_mfma_f32_16x16x32_bf16 v[128:131], a[96:99], v[160:163], v[128:131]\n"
"v_cvt_pk_bf16_f32 v103, v106, v107\n"
"v_permlane16_swap_b32 v100, v102\n"
"v_mfma_f32_16x16x32_bf16 v[128:131], a[104:107], v[164:167], v[128:131]\n"
"v_permlane16_swap_b32 v101, v103\n"
"buffer_store_dwordx4 v[100:103], v1, s[40:43], s51 offen sc1\n"
"v_mfma_f32_16x16x32_bf16 v[128:131], a[112:115], v[168:171], v[128:131]\n"
"s_add_u32 m0, s75, 98304\n"
"s_add_u32 s63, s45, s46\n"
"s_add_u32 s64, s51, 114688\n"
"v_mfma_f32_16x16x32_bf16 v[128:131], a[120:123], v[172:175], v[128:131]\n"
"ds_read_b64_tr_b16 a[150:151], v232 offset:49672\n"
"ds_read_b64_tr_b16 a[152:153], v233 offset:49152\n"
"ds_read_b64_tr_b16 a[154:155], v233 offset:49664\n"
"v_mfma_f32_16x16x32_bf16 v[132:135], a[100:103], v[160:163], v[132:135]\n"
"ds_read_b128 v[160:163], v14 offset:59392\n"
"v_max_f32 v112, 0, v112\n"
"ds_read_b64_tr_b16 a[156:157], v233 offset:49160\n"
"v_mfma_f32_16x16x32_bf16 v[132:135], a[108:111], v[164:167], v[132:135]\n"
"ds_read_b128 v[164:167], v15 offset:59392\n"
"v_mul_f32 v112, v112, v116\n"
"v_mfma_f32_16x16x32_bf16 v[132:135], a[116:119], v[168:171], v[132:135]\n"
"ds_read_b128 v[168:171], v14 offset:63488\n"
"v_max_f32 v113, 0, v113\n"
"v_mul_f32 v113, v113, v117\n"
"v_mfma_f32_16x16x32_bf16 v[132:135], a[124:127], v[172:175], v[132:135]\n"
"ds_read_b128 v[172:175], v15 offset:63488\n"
"buffer_load_dwordx4 v6, s[8:11], s63 offen offset:0 lds\n"
"s_waitcnt lgkmcnt(8)\n"
"v_mfma_f32_16x16x32_bf16 v[140:143], a[68:71], v[144:147], v[140:143]\n"
"v_max_f32 v114, 0, v114\n"
"v_mul_f32 v114, v114, v118\n"
"v_max_f32 v115, 0, v115\n"
"v_mfma_f32_16x16x32_bf16 v[140:143], a[76:79], v[148:151], v[140:143]\n"
"v_mul_f32 v115, v115, v119\n"
"v_cvt_pk_bf16_f32 v116, v112, v113\n"
"v_cvt_pk_bf16_f32 v117, v114, v115\n"
"v_mfma_f32_16x16x32_bf16 v[140:143], a[84:87], v[152:155], v[140:143]\n"
"buffer_load_dwordx4 v7, s[8:11], s63 offen offset:1024 lds\n"
"ds_read_b64_tr_b16 a[158:159], v233 offset:49672\n"
"v_mfma_f32_16x16x32_bf16 v[140:143], a[92:95], v[156:159], v[140:143]\n"
"v_mfma_f32_16x16x32_bf16 v[136:139], a[64:67], v[144:147], v[136:139]\n"
"v_mfma_f32_16x16x32_bf16 v[136:139], a[72:75], v[148:151], v[136:139]\n"
"v_mfma_f32_16x16x32_bf16 v[136:139], a[80:83], v[152:155], v[136:139]\n"
"s_waitcnt vmcnt(13) lgkmcnt(1)\n"
"s_barrier\n"
"v_mfma_f32_16x16x32_bf16 v[136:139], a[88:91], v[156:159], v[136:139]\n"
"ds_read_b128 v[144:147], v14 offset:0\n"
"ds_read_b128 v[148:151], v15 offset:0\n"
"v_mfma_f32_16x16x32_bf16 v[136:139], a[96:99], v[160:163], v[136:139]\n"
"ds_read_b128 v[152:155], v14 offset:4096\n"
"v_max_f32 v120, 0, v120\n"
"v_mul_f32 v120, v120, v124\n"
"v_mfma_f32_16x16x32_bf16 v[136:139], a[104:107], v[164:167], v[136:139]\n"
"ds_read_b128 v[156:159], v15 offset:4096\n"
"v_max_f32 v121, 0, v121\n"
"v_mul_f32 v121, v121, v125\n"
"v_mfma_f32_16x16x32_bf16 v[136:139], a[112:115], v[168:171], v[136:139]\n"
"buffer_load_dwordx4 v8, s[8:11], s63 offen offset:2048 lds\n"
"v_max_f32 v122, 0, v122\n"
"v_mfma_f32_16x16x32_bf16 v[136:139], a[120:123], v[172:175], v[136:139]\n"
"v_mul_f32 v122, v122, v126\n"
"v_max_f32 v123, 0, v123\n"
"v_mul_f32 v123, v123, v127\n"
"v_mfma_f32_16x16x32_bf16 v[140:143], a[100:103], v[160:163], v[140:143]\n"
"ds_read_b128 v[160:163], v14 offset:8192\n"
"v_cvt_pk_bf16_f32 v118, v120, v121\n"
"v_cvt_pk_bf16_f32 v119, v122, v123\n"
"v_mfma_f32_16x16x32_bf16 v[140:143], a[108:111], v[164:167], v[140:143]\n"
"ds_read_b128 v[164:167], v15 offset:8192\n"
"v_permlane16_swap_b32 v116, v118\n"
"v_mfma_f32_16x16x32_bf16 v[140:143], a[116:119], v[168:171], v[140:143]\n"
"ds_read_b128 v[168:171], v14 offset:12288\n"
"v_permlane16_swap_b32 v117, v119\n"
"v_mfma_f32_16x16x32_bf16 v[140:143], a[124:127], v[172:175], v[140:143]\n"
"ds_read_b128 v[172:175], v15 offset:12288\n"
"buffer_store_dwordx4 v[116:119], v1, s[40:43], s64 offen sc1\n"
"v_mfma_f32_16x16x32_bf16 v[64:67], a[240:243], v[50:53], 0\n"
"buffer_load_dwordx4 v9, s[8:11], s63 offen offset:3072 lds\n"
"v_max_f32 v128, 0, v128\n"
"v_mfma_f32_16x16x32_bf16 v[68:71], a[244:247], v[50:53], 0\n"
"v_mul_f32 v128, v128, v132\n"
"v_max_f32 v129, 0, v129\n"
"v_mul_f32 v129, v129, v133\n"
"s_waitcnt lgkmcnt(4)\n"
"v_mfma_f32_16x16x32_bf16 v[68:71], a[4:7], v[144:147], v[68:71]\n"
"v_max_f32 v130, 0, v130\n"
"v_mul_f32 v130, v130, v134\n"
"v_max_f32 v131, 0, v131\n"
"v_mfma_f32_16x16x32_bf16 v[68:71], a[12:15], v[148:151], v[68:71]\n"
"v_mul_f32 v131, v131, v135\n"
"v_cvt_pk_bf16_f32 v132, v128, v129\n"
"v_cvt_pk_bf16_f32 v133, v130, v131\n"
"v_mfma_f32_16x16x32_bf16 v[68:71], a[20:23], v[152:155], v[68:71]\n"
"s_add_u32 m0, s95, 4160\n"
"s_add_u32 s63, s96, 4\n"
"v_max_f32 v136, 0, v136\n"
"v_mfma_f32_16x16x32_bf16 v[68:71], a[28:31], v[156:159], v[68:71]\n"
"v_mul_f32 v136, v136, v140\n"
"v_max_f32 v137, 0, v137\n"
"v_mul_f32 v137, v137, v141\n"
"v_mfma_f32_16x16x32_bf16 v[64:67], a[0:3], v[144:147], v[64:67]\n"
"ds_read_b128 v[144:147], v14 offset:2048\n"
"v_max_f32 v138, 0, v138\n"
"v_mul_f32 v138, v138, v142\n"
"v_mfma_f32_16x16x32_bf16 v[64:67], a[8:11], v[148:151], v[64:67]\n"
"ds_read_b128 v[148:151], v15 offset:2048\n"
"v_max_f32 v139, 0, v139\n"
"v_mul_f32 v139, v139, v143\n"
"v_mfma_f32_16x16x32_bf16 v[64:67], a[16:19], v[152:155], v[64:67]\n"
"ds_read_b128 v[152:155], v14 offset:6144\n"
"v_cvt_pk_bf16_f32 v134, v136, v137\n"
"v_cvt_pk_bf16_f32 v135, v138, v139\n"
"v_mfma_f32_16x16x32_bf16 v[64:67], a[24:27], v[156:159], v[64:67]\n"
"ds_read_b128 v[156:159], v15 offset:6144\n"
"buffer_load_dwordx4 v1, s[28:31], s63 offen offset:0 lds\n"
"s_waitcnt lgkmcnt(4)\n"
"v_mfma_f32_16x16x32_bf16 v[64:67], a[32:35], v[160:163], v[64:67]\n"
"buffer_load_dwordx4 v1, s[28:31], s63 offen offset:1024 lds\n"
"v_permlane16_swap_b32 v132, v134\n"
"v_mfma_f32_16x16x32_bf16 v[64:67], a[40:43], v[164:167], v[64:67]\n"
"v_permlane16_swap_b32 v133, v135\n"
"v_mfma_f32_16x16x32_bf16 v[64:67], a[48:51], v[168:171], v[64:67]\n"
"v_mfma_f32_16x16x32_bf16 v[64:67], a[56:59], v[172:175], v[64:67]\n"
"v_mfma_f32_16x16x32_bf16 v[68:71], a[36:39], v[160:163], v[68:71]\n"
"ds_read_b128 v[160:163], v14 offset:10240\n"
"v_mfma_f32_16x16x32_bf16 v[68:71], a[44:47], v[164:167], v[68:71]\n"
"ds_read_b128 v[164:167], v15 offset:10240\n"
"v_mfma_f32_16x16x32_bf16 v[68:71], a[52:55], v[168:171], v[68:71]\n"
"ds_read_b128 v[168:171], v14 offset:14336\n"
"buffer_store_dwordx4 v[132:135], v1, s[40:43], s52 offen sc1\n"
"v_mfma_f32_16x16x32_bf16 v[68:71], a[60:63], v[172:175], v[68:71]\n"
"ds_read_b128 v[172:175], v15 offset:14336\n"
"buffer_load_dwordx4 v1, s[28:31], s63 offen offset:2048 lds\n"
"v_mfma_f32_16x16x32_bf16 v[72:75], a[248:251], v[50:53], 0\n"
"v_mfma_f32_16x16x32_bf16 v[76:79], a[252:255], v[50:53], 0\n"
"s_waitcnt lgkmcnt(4)\n"
"v_mfma_f32_16x16x32_bf16 v[76:79], a[4:7], v[144:147], v[76:79]\n"
"v_mfma_f32_16x16x32_bf16 v[76:79], a[12:15], v[148:151], v[76:79]\n"
"buffer_load_dwordx4 v1, s[28:31], s63 offen offset:3072 lds\n"
"v_mfma_f32_16x16x32_bf16 v[76:79], a[20:23], v[152:155], v[76:79]\n"
"s_add_u32 m0, s75, 114688\n"
"s_add_u32 s63, s45, s47\n"
"v_mfma_f32_16x16x32_bf16 v[76:79], a[28:31], v[156:159], v[76:79]\n"
"v_mfma_f32_16x16x32_bf16 v[72:75], a[0:3], v[144:147], v[72:75]\n"
"v_mfma_f32_16x16x32_bf16 v[72:75], a[8:11], v[148:151], v[72:75]\n"
"buffer_load_dwordx4 v6, s[8:11], s63 offen offset:0 lds\n"
"v_mfma_f32_16x16x32_bf16 v[72:75], a[16:19], v[152:155], v[72:75]\n"
"s_waitcnt vmcnt(12) lgkmcnt(0)\n"
"s_barrier\n"
"v_mfma_f32_16x16x32_bf16 v[72:75], a[24:27], v[156:159], v[72:75]\n"
"ds_read_b128 v[144:147], v14 offset:16384\n"
"ds_read_b128 v[148:151], v15 offset:16384\n"
"v_mfma_f32_16x16x32_bf16 v[72:75], a[32:35], v[160:163], v[72:75]\n"
"ds_read_b128 v[152:155], v14 offset:20480\n"
"buffer_load_dword v10, v228, s[28:31], s5 offen\n"
"v_mfma_f32_16x16x32_bf16 v[72:75], a[40:43], v[164:167], v[72:75]\n"
"ds_read_b128 v[156:159], v15 offset:20480\n"
"v_mfma_f32_16x16x32_bf16 v[72:75], a[48:51], v[168:171], v[72:75]\n"
"buffer_load_dwordx4 v7, s[8:11], s63 offen offset:1024 lds\n"
"v_mfma_f32_16x16x32_bf16 v[72:75], a[56:59], v[172:175], v[72:75]\n"
"v_mfma_f32_16x16x32_bf16 v[76:79], a[36:39], v[160:163], v[76:79]\n"
"ds_read_b128 v[160:163], v14 offset:24576\n"
"v_mfma_f32_16x16x32_bf16 v[76:79], a[44:47], v[164:167], v[76:79]\n"
"ds_read_b128 v[164:167], v15 offset:24576\n"
"v_mfma_f32_16x16x32_bf16 v[76:79], a[52:55], v[168:171], v[76:79]\n"
"ds_read_b128 v[168:171], v14 offset:28672\n"
"buffer_load_dword v11, v229, s[28:31], s5 offen\n"
"v_mfma_f32_16x16x32_bf16 v[76:79], a[60:63], v[172:175], v[76:79]\n"
"ds_read_b128 v[172:175], v15 offset:28672\n"
"s_waitcnt lgkmcnt(7)\n"
"v_mfma_f32_16x16x32_bf16 v[68:71], a[68:71], v[144:147], v[68:71]\n"
"buffer_load_dword v12, v230, s[28:31], s5 offen\n"
"s_waitcnt lgkmcnt(6)\n"
"v_mfma_f32_16x16x32_bf16 v[68:71], a[76:79], v[148:151], v[68:71]\n"
"s_waitcnt lgkmcnt(5)\n"
"v_mfma_f32_16x16x32_bf16 v[68:71], a[84:87], v[152:155], v[68:71]\n"
"buffer_load_dword v13, v231, s[28:31], s5 offen\n"
"s_waitcnt lgkmcnt(4)\n"
"v_mfma_f32_16x16x32_bf16 v[68:71], a[92:95], v[156:159], v[68:71]\n"
"buffer_load_dwordx4 v8, s[8:11], s63 offen offset:2048 lds\n"
"v_mfma_f32_16x16x32_bf16 v[64:67], a[64:67], v[144:147], v[64:67]\n"
"ds_read_b128 v[144:147], v14 offset:18432\n"
"v_mfma_f32_16x16x32_bf16 v[64:67], a[72:75], v[148:151], v[64:67]\n"
"ds_read_b128 v[148:151], v15 offset:18432\n"
"v_mfma_f32_16x16x32_bf16 v[64:67], a[80:83], v[152:155], v[64:67]\n"
"ds_read_b128 v[152:155], v14 offset:22528\n"
"v_mfma_f32_16x16x32_bf16 v[64:67], a[88:91], v[156:159], v[64:67]\n"
"ds_read_b128 v[156:159], v15 offset:22528\n"
"s_waitcnt lgkmcnt(4)\n"
"v_mfma_f32_16x16x32_bf16 v[64:67], a[96:99], v[160:163], v[64:67]\n"
"buffer_load_dwordx4 v9, s[8:11], s63 offen offset:3072 lds\n"
"v_mfma_f32_16x16x32_bf16 v[64:67], a[104:107], v[164:167], v[64:67]\n"
"v_mfma_f32_16x16x32_bf16 v[64:67], a[112:115], v[168:171], v[64:67]\n"
"v_mfma_f32_16x16x32_bf16 v[64:67], a[120:123], v[172:175], v[64:67]\n"
"v_mfma_f32_16x16x32_bf16 v[68:71], a[100:103], v[160:163], v[68:71]\n"
"ds_read_b128 v[160:163], v14 offset:26624\n"
"s_add_u32 s64, s52, 114688\n"
"v_mfma_f32_16x16x32_bf16 v[68:71], a[108:111], v[164:167], v[68:71]\n"
"ds_read_b128 v[164:167], v15 offset:26624\n"
"v_mfma_f32_16x16x32_bf16 v[68:71], a[116:119], v[168:171], v[68:71]\n"
"ds_read_b128 v[168:171], v14 offset:30720\n"
"s_add_u32 m0, s75, 65536\n"
"v_mfma_f32_16x16x32_bf16 v[68:71], a[124:127], v[172:175], v[68:71]\n"
"ds_read_b128 v[172:175], v15 offset:30720\n"
"s_waitcnt lgkmcnt(5)\n"
"v_mfma_f32_16x16x32_bf16 v[76:79], a[68:71], v[144:147], v[76:79]\n"
"buffer_load_dwordx2 v[38:39], v62, s[20:23], 0 offen offset:0 sc1\n"
"v_mfma_f32_16x16x32_bf16 v[76:79], a[76:79], v[148:151], v[76:79]\n"
"s_add_u32 s63, s45, s48\n"
"v_mfma_f32_16x16x32_bf16 v[76:79], a[84:87], v[152:155], v[76:79]\n"
"s_waitcnt vmcnt(15)\n"
"buffer_store_dwordx2 v[2:3], v4, s[40:43], s54 offen offset:0 sc1\n"
"s_waitcnt lgkmcnt(4)\n"
"v_mfma_f32_16x16x32_bf16 v[76:79], a[92:95], v[156:159], v[76:79]\n"
"buffer_load_dwordx4 v6, s[8:11], s63 offen offset:0 lds\n"
"v_mfma_f32_16x16x32_bf16 v[72:75], a[64:67], v[144:147], v[72:75]\n"
"v_max_f32 v64, 0, v64\n"
"v_mul_f32 v64, v64, v68\n"
"v_max_f32 v65, 0, v65\n"
"v_mfma_f32_16x16x32_bf16 v[72:75], a[72:75], v[148:151], v[72:75]\n"
"v_mul_f32 v65, v65, v69\n"
"v_max_f32 v66, 0, v66\n"
"v_mul_f32 v66, v66, v70\n"
"v_mfma_f32_16x16x32_bf16 v[72:75], a[80:83], v[152:155], v[72:75]\n"
"s_waitcnt vmcnt(16) lgkmcnt(0)\n"
"s_barrier\n"
"v_mfma_f32_16x16x32_bf16 v[72:75], a[88:91], v[156:159], v[72:75]\n"
"ds_read_b128 v[144:147], v14 offset:32768\n"
"ds_read_b128 v[148:151], v15 offset:32768\n"
"v_max_f32 v67, 0, v67\n"
"v_mfma_f32_16x16x32_bf16 v[72:75], a[96:99], v[160:163], v[72:75]\n"
"ds_read_b128 v[152:155], v14 offset:36864\n"
"v_mul_f32 v67, v67, v71\n"
"v_cvt_pk_bf16_f32 v68, v64, v65\n"
"v_mfma_f32_16x16x32_bf16 v[72:75], a[104:107], v[164:167], v[72:75]\n"
"ds_read_b128 v[156:159], v15 offset:36864\n"
"buffer_load_dwordx4 v7, s[8:11], s63 offen offset:1024 lds\n"
"v_mfma_f32_16x16x32_bf16 v[72:75], a[112:115], v[168:171], v[72:75]\n"
"v_cvt_pk_bf16_f32 v69, v66, v67\n"
"v_mfma_f32_16x16x32_bf16 v[72:75], a[120:123], v[172:175], v[72:75]\n"
"v_mfma_f32_16x16x32_bf16 v[76:79], a[100:103], v[160:163], v[76:79]\n"
"ds_read_b128 v[160:163], v14 offset:40960\n"
"v_mfma_f32_16x16x32_bf16 v[76:79], a[108:111], v[164:167], v[76:79]\n"
"ds_read_b128 v[164:167], v15 offset:40960\n"
"v_mfma_f32_16x16x32_bf16 v[76:79], a[116:119], v[168:171], v[76:79]\n"
"ds_read_b128 v[168:171], v14 offset:45056\n"
"v_mfma_f32_16x16x32_bf16 v[76:79], a[124:127], v[172:175], v[76:79]\n"
"ds_read_b128 v[172:175], v15 offset:45056\n"
"buffer_load_dwordx4 v8, s[8:11], s63 offen offset:2048 lds\n"
"v_mfma_f32_16x16x32_bf16 v[80:83], a[240:243], v[52:55], 0\n"
"v_mfma_f32_16x16x32_bf16 v[84:87], a[244:247], v[52:55], 0\n"
"s_waitcnt lgkmcnt(5)\n"
"v_mfma_f32_16x16x32_bf16 v[84:87], a[4:7], v[144:147], v[84:87]\n"
"v_max_f32 v72, 0, v72\n"
"v_mfma_f32_16x16x32_bf16 v[84:87], a[12:15], v[148:151], v[84:87]\n"
"v_mul_f32 v72, v72, v76\n"
"v_max_f32 v73, 0, v73\n"
"v_mfma_f32_16x16x32_bf16 v[84:87], a[20:23], v[152:155], v[84:87]\n"
"v_mul_f32 v73, v73, v77\n"
"v_max_f32 v74, 0, v74\n"
"v_mul_f32 v74, v74, v78\n"
"s_waitcnt lgkmcnt(3)\n"
"v_mfma_f32_16x16x32_bf16 v[84:87], a[28:31], v[156:159], v[84:87]\n"
"buffer_load_dwordx4 v9, s[8:11], s63 offen offset:3072 lds\n"
"v_max_f32 v75, 0, v75\n"
"v_mfma_f32_16x16x32_bf16 v[80:83], a[0:3], v[144:147], v[80:83]\n"
"ds_read_b128 v[144:147], v14 offset:34816\n"
"v_mul_f32 v75, v75, v79\n"
"v_cvt_pk_bf16_f32 v70, v72, v73\n"
"v_mfma_f32_16x16x32_bf16 v[80:83], a[8:11], v[148:151], v[80:83]\n"
"ds_read_b128 v[148:151], v15 offset:34816\n"
"v_cvt_pk_bf16_f32 v71, v74, v75\n"
"v_permlane16_swap_b32 v68, v70\n"
"v_mfma_f32_16x16x32_bf16 v[80:83], a[16:19], v[152:155], v[80:83]\n"
"ds_read_b128 v[152:155], v14 offset:38912\n"
"v_permlane16_swap_b32 v69, v71\n"
"v_mfma_f32_16x16x32_bf16 v[80:83], a[24:27], v[156:159], v[80:83]\n"
"ds_read_b128 v[156:159], v15 offset:38912\n"
"buffer_store_dwordx4 v[68:71], v1, s[40:43], s64 offen sc1\n"
"v_mfma_f32_16x16x32_bf16 v[80:83], a[32:35], v[160:163], v[80:83]\n"
"s_waitcnt lgkmcnt(4)\n"
"v_mfma_f32_16x16x32_bf16 v[80:83], a[40:43], v[164:167], v[80:83]\n"
"v_mfma_f32_16x16x32_bf16 v[80:83], a[48:51], v[168:171], v[80:83]\n"
"s_add_u32 m0, s75, 81920\n"
"s_add_u32 s63, s45, s49\n"
"v_mfma_f32_16x16x32_bf16 v[80:83], a[56:59], v[172:175], v[80:83]\n"
"v_mfma_f32_16x16x32_bf16 v[84:87], a[36:39], v[160:163], v[84:87]\n"
"ds_read_b128 v[160:163], v14 offset:43008\n"
"v_mfma_f32_16x16x32_bf16 v[84:87], a[44:47], v[164:167], v[84:87]\n"
"ds_read_b128 v[164:167], v15 offset:43008\n"
"v_mfma_f32_16x16x32_bf16 v[84:87], a[52:55], v[168:171], v[84:87]\n"
"ds_read_b128 v[168:171], v14 offset:47104\n"
"v_mfma_f32_16x16x32_bf16 v[84:87], a[60:63], v[172:175], v[84:87]\n"
"ds_read_b128 v[172:175], v15 offset:47104\n"
"buffer_load_dwordx4 v6, s[8:11], s63 offen offset:0 lds\n"
"v_mfma_f32_16x16x32_bf16 v[88:91], a[248:251], v[52:55], 0\n"
"v_mfma_f32_16x16x32_bf16 v[92:95], a[252:255], v[52:55], 0\n"
"s_waitcnt lgkmcnt(4)\n"
"v_mfma_f32_16x16x32_bf16 v[92:95], a[4:7], v[144:147], v[92:95]\n"
"v_mfma_f32_16x16x32_bf16 v[92:95], a[12:15], v[148:151], v[92:95]\n"
"v_mfma_f32_16x16x32_bf16 v[92:95], a[20:23], v[152:155], v[92:95]\n"
"buffer_load_dwordx4 v7, s[8:11], s63 offen offset:1024 lds\n"
"v_mfma_f32_16x16x32_bf16 v[92:95], a[28:31], v[156:159], v[92:95]\n"
"v_mfma_f32_16x16x32_bf16 v[88:91], a[0:3], v[144:147], v[88:91]\n"
"v_mfma_f32_16x16x32_bf16 v[88:91], a[8:11], v[148:151], v[88:91]\n"
"v_mfma_f32_16x16x32_bf16 v[88:91], a[16:19], v[152:155], v[88:91]\n"
"s_waitcnt vmcnt(9) lgkmcnt(0)\n"
"s_barrier\n"
"v_mfma_f32_16x16x32_bf16 v[88:91], a[24:27], v[156:159], v[88:91]\n"
"ds_read_b128 v[144:147], v14 offset:49152\n"
"ds_read_b128 v[148:151], v15 offset:49152\n"
"s_waitcnt vmcnt(8)\n"
"v_cmp_ne_u64 vcc, v[38:39], v[2:3]\n"
"s_cbranch_vccnz LslowK_m_%=\n"
"LokK_m_%=:\n"
"v_mfma_f32_16x16x32_bf16 v[88:91], a[32:35], v[160:163], v[88:91]\n"
"ds_read_b128 v[152:155], v14 offset:53248\n"
"ds_read_b128 v[156:159], v15 offset:53248\n"
"buffer_load_dwordx4 v[192:195], v234, s[20:23], 0 offen offset:1024 sc1\n"
"v_mfma_f32_16x16x32_bf16 v[88:91], a[40:43], v[164:167], v[88:91]\n"
"v_mfma_f32_16x16x32_bf16 v[88:91], a[48:51], v[168:171], v[88:91]\n"
"v_mfma_f32_16x16x32_bf16 v[88:91], a[56:59], v[172:175], v[88:91]\n"
"buffer_load_dwordx4 v[196:199], v234, s[20:23], 0 offen sc1\n"
"v_mfma_f32_16x16x32_bf16 v[92:95], a[36:39], v[160:163], v[92:95]\n"
"ds_read_b128 v[160:163], v14 offset:57344\n"
"v_mfma_f32_16x16x32_bf16 v[92:95], a[44:47], v[164:167], v[92:95]\n"
"ds_read_b128 v[164:167], v15 offset:57344\n"
"v_mfma_f32_16x16x32_bf16 v[92:95], a[52:55], v[168:171], v[92:95]\n"
"ds_read_b128 v[168:171], v14 offset:61440\n"
"v_mfma_f32_16x16x32_bf16 v[92:95], a[60:63], v[172:175], v[92:95]\n"
"ds_read_b128 v[172:175], v15 offset:61440\n"
"s_waitcnt lgkmcnt(7)\n"
"v_mfma_f32_16x16x32_bf16 v[84:87], a[68:71], v[144:147], v[84:87]\n"
"s_waitcnt lgkmcnt(6)\n"
"v_mfma_f32_16x16x32_bf16 v[84:87], a[76:79], v[148:151], v[84:87]\n"
"buffer_load_dword v240, v240, s[20:23], 0 offen offset:1024 sc1\n"
"s_waitcnt lgkmcnt(5)\n"
"v_mfma_f32_16x16x32_bf16 v[84:87], a[84:87], v[152:155], v[84:87]\n"
"s_waitcnt lgkmcnt(4)\n"
"v_mfma_f32_16x16x32_bf16 v[84:87], a[92:95], v[156:159], v[84:87]\n"
"v_mfma_f32_16x16x32_bf16 v[80:83], a[64:67], v[144:147], v[80:83]\n"
"ds_read_b128 v[144:147], v14 offset:51200\n"
"v_mfma_f32_16x16x32_bf16 v[80:83], a[72:75], v[148:151], v[80:83]\n"
"ds_read_b128 v[148:151], v15 offset:51200\n"
"v_mfma_f32_16x16x32_bf16 v[80:83], a[80:83], v[152:155], v[80:83]\n"
"ds_read_b128 v[152:155], v14 offset:55296\n"
"buffer_load_dword v241, v241, s[20:23], 0 offen sc1\n"
"v_mfma_f32_16x16x32_bf16 v[80:83], a[88:91], v[156:159], v[80:83]\n"
"ds_read_b128 v[156:159], v15 offset:55296\n"
"s_waitcnt lgkmcnt(4)\n"
"v_mfma_f32_16x16x32_bf16 v[80:83], a[96:99], v[160:163], v[80:83]\n"
"v_mfma_f32_16x16x32_bf16 v[80:83], a[104:107], v[164:167], v[80:83]\n"
"v_mfma_f32_16x16x32_bf16 v[80:83], a[112:115], v[168:171], v[80:83]\n"
"v_mfma_f32_16x16x32_bf16 v[80:83], a[120:123], v[172:175], v[80:83]\n"
"v_mfma_f32_16x16x32_bf16 v[84:87], a[100:103], v[160:163], v[84:87]\n"
"ds_read_b128 v[160:163], v14 offset:59392\n"
"buffer_load_dwordx4 v8, s[8:11], s63 offen offset:2048 lds\n"
"v_mfma_f32_16x16x32_bf16 v[84:87], a[108:111], v[164:167], v[84:87]\n"
"ds_read_b128 v[164:167], v15 offset:59392\n"
"v_mfma_f32_16x16x32_bf16 v[84:87], a[116:119], v[168:171], v[84:87]\n"
"ds_read_b128 v[168:171], v14 offset:63488\n"
"v_mfma_f32_16x16x32_bf16 v[84:87], a[124:127], v[172:175], v[84:87]\n"
"ds_read_b128 v[172:175], v15 offset:63488\n"
"s_waitcnt lgkmcnt(5)\n"
"v_mfma_f32_16x16x32_bf16 v[92:95], a[68:71], v[144:147], v[92:95]\n"
"buffer_load_dwordx4 v9, s[8:11], s63 offen offset:3072 lds\n"
"s_add_u32 m0, s75, 131072\n"
"s_lshl_b32 s63, s90, 6\n"
"v_mfma_f32_16x16x32_bf16 v[92:95], a[76:79], v[148:151], v[92:95]\n"
"v_mfma_f32_16x16x32_bf16 v[92:95], a[84:87], v[152:155], v[92:95]\n"
"v_max_f32 v80, 0, v80\n"
"s_waitcnt lgkmcnt(4)\n"
"v_mfma_f32_16x16x32_bf16 v[92:95], a[92:95], v[156:159], v[92:95]\n"
"v_mul_f32 v80, v80, v84\n"
"buffer_load_dwordx4 v18, s[24:27], s63 offen offset:0 lds\n"
"v_mfma_f32_16x16x32_bf16 v[88:91], a[64:67], v[144:147], v[88:91]\n"
"v_max_f32 v81, 0, v81\n"
"v_mul_f32 v81, v81, v85\n"
"v_max_f32 v82, 0, v82\n"
"v_mfma_f32_16x16x32_bf16 v[88:91], a[72:75], v[148:151], v[88:91]\n"
"v_mul_f32 v82, v82, v86\n"
"v_max_f32 v83, 0, v83\n"
"v_mul_f32 v83, v83, v87\n"
"v_mfma_f32_16x16x32_bf16 v[88:91], a[80:83], v[152:155], v[88:91]\n"
"s_waitcnt vmcnt(10) lgkmcnt(0)\n"
"s_barrier\n"
"buffer_load_dwordx4 v19, s[24:27], s63 offen offset:1024 lds\n"
"v_mfma_f32_16x16x32_bf16 v[88:91], a[88:91], v[156:159], v[88:91]\n"
"ds_read_b128 v[144:147], v14 offset:0\n"
"ds_read_b128 v[148:151], v15 offset:0\n"
"v_cvt_pk_bf16_f32 v84, v80, v81\n"
"v_mfma_f32_16x16x32_bf16 v[88:91], a[96:99], v[160:163], v[88:91]\n"
"ds_read_b128 v[152:155], v14 offset:4096\n"
"v_cvt_pk_bf16_f32 v85, v82, v83\n"
"v_mfma_f32_16x16x32_bf16 v[88:91], a[104:107], v[164:167], v[88:91]\n"
"ds_read_b128 v[156:159], v15 offset:4096\n"
"v_mov_b32 v200, 0\n"
"buffer_load_dwordx4 v20, s[24:27], s63 offen offset:2048 lds\n"
"v_mfma_f32_16x16x32_bf16 v[88:91], a[112:115], v[168:171], v[88:91]\n"
"v_mov_b32 v201, 0\n"
"v_mov_b32 v202, 0\n"
"v_mfma_f32_16x16x32_bf16 v[88:91], a[120:123], v[172:175], v[88:91]\n"
"v_mov_b32 v203, 0\n"
"v_mfma_f32_16x16x32_bf16 v[92:95], a[100:103], v[160:163], v[92:95]\n"
"ds_read_b128 v[160:163], v14 offset:8192\n"
"v_mfma_f32_16x16x32_bf16 v[92:95], a[108:111], v[164:167], v[92:95]\n"
"ds_read_b128 v[164:167], v15 offset:8192\n"
"v_mfma_f32_16x16x32_bf16 v[92:95], a[116:119], v[168:171], v[92:95]\n"
"ds_read_b128 v[168:171], v14 offset:12288\n"
"buffer_load_dwordx4 v21, s[24:27], s63 offen offset:3072 lds\n"
"v_mfma_f32_16x16x32_bf16 v[92:95], a[124:127], v[172:175], v[92:95]\n"
"ds_read_b128 v[172:175], v15 offset:12288\n"
"v_mfma_f32_16x16x32_bf16 v[96:99], a[240:243], v[54:57], 0\n"
"ds_write_b128 v243, v[200:203] offset:4096\n"
"v_mfma_f32_16x16x32_bf16 v[100:103], a[244:247], v[54:57], 0\n"
"s_waitcnt lgkmcnt(5)\n"
"v_mfma_f32_16x16x32_bf16 v[100:103], a[4:7], v[144:147], v[100:103]\n"
"s_add_u32 m0, s75, 147456\n"
"s_lshl_b32 s63, s91, 6\n"
"v_mfma_f32_16x16x32_bf16 v[100:103], a[12:15], v[148:151], v[100:103]\n"
"v_max_f32 v88, 0, v88\n"
"v_mul_f32 v88, v88, v92\n"
"v_mfma_f32_16x16x32_bf16 v[100:103], a[20:23], v[152:155], v[100:103]\n"
"buffer_load_dwordx4 v18, s[24:27], s63 offen offset:0 lds\n"
"v_max_f32 v89, 0, v89\n"
"v_mfma_f32_16x16x32_bf16 v[100:103], a[28:31], v[156:159], v[100:103]\n"
"v_mul_f32 v89, v89, v93\n"
"v_max_f32 v90, 0, v90\n"
"v_mul_f32 v90, v90, v94\n"
"v_mfma_f32_16x16x32_bf16 v[96:99], a[0:3], v[144:147], v[96:99]\n"
"ds_read_b128 v[144:147], v14 offset:2048\n"
"v_max_f32 v91, 0, v91\n"
"v_mul_f32 v91, v91, v95\n"
"v_mfma_f32_16x16x32_bf16 v[96:99], a[8:11], v[148:151], v[96:99]\n"
"ds_read_b128 v[148:151], v15 offset:2048\n"
"v_cvt_pk_bf16_f32 v86, v88, v89\n"
"v_cvt_pk_bf16_f32 v87, v90, v91\n"
"v_mfma_f32_16x16x32_bf16 v[96:99], a[16:19], v[152:155], v[96:99]\n"
"ds_read_b128 v[152:155], v14 offset:6144\n"
"buffer_load_dwordx2 v[38:39], v63, s[20:23], 0 offen offset:0 sc1\n"
"v_mfma_f32_16x16x32_bf16 v[96:99], a[24:27], v[156:159], v[96:99]\n"
"ds_read_b128 v[156:159], v15 offset:6144\n"
"v_permlane16_swap_b32 v84, v86\n"
"s_waitcnt lgkmcnt(5)\n"
"v_mfma_f32_16x16x32_bf16 v[96:99], a[32:35], v[160:163], v[96:99]\n"
"buffer_load_dwordx4 v19, s[24:27], s63 offen offset:1024 lds\n"
"v_permlane16_swap_b32 v85, v87\n"
"v_mfma_f32_16x16x32_bf16 v[96:99], a[40:43], v[164:167], v[96:99]\n"
"v_mfma_f32_16x16x32_bf16 v[96:99], a[48:51], v[168:171], v[96:99]\n"
"v_mfma_f32_16x16x32_bf16 v[96:99], a[56:59], v[172:175], v[96:99]\n"
"v_mfma_f32_16x16x32_bf16 v[100:103], a[36:39], v[160:163], v[100:103]\n"
"ds_read_b128 v[160:163], v14 offset:10240\n"
"buffer_store_dwordx4 v[84:87], v1, s[40:43], s53 offen sc1\n"
"v_mfma_f32_16x16x32_bf16 v[100:103], a[44:47], v[164:167], v[100:103]\n"
"ds_read_b128 v[164:167], v15 offset:10240\n"
"v_mfma_f32_16x16x32_bf16 v[100:103], a[52:55], v[168:171], v[100:103]\n"
"ds_read_b128 v[168:171], v14 offset:14336\n"
"v_mfma_f32_16x16x32_bf16 v[100:103], a[60:63], v[172:175], v[100:103]\n"
"ds_read_b128 v[172:175], v15 offset:14336\n"
"buffer_load_dwordx4 v20, s[24:27], s63 offen offset:2048 lds\n"
"v_mfma_f32_16x16x32_bf16 v[104:107], a[248:251], v[54:57], 0\n"
"s_waitcnt vmcnt(11)\n"
"ds_write_b128 v242, v[192:195] offset:4576\n"
"v_mfma_f32_16x16x32_bf16 v[108:111], a[252:255], v[54:57], 0\n"
"v_alignbit_b32 v204, v193, v192, 16\n"
"s_waitcnt lgkmcnt(5)\n"
"v_mfma_f32_16x16x32_bf16 v[108:111], a[4:7], v[144:147], v[108:111]\n"
"v_alignbit_b32 v205, v194, v193, 16\n"
"v_alignbit_b32 v206, v195, v194, 16\n"
"v_mfma_f32_16x16x32_bf16 v[108:111], a[12:15], v[148:151], v[108:111]\n"
"buffer_load_dwordx4 v21, s[24:27], s63 offen offset:3072 lds\n"
"v_mfma_f32_16x16x32_bf16 v[108:111], a[20:23], v[152:155], v[108:111]\n"
"v_alignbit_b32 v207, v240, v195, 16\n"
"v_mfma_f32_16x16x32_bf16 v[108:111], a[28:31], v[156:159], v[108:111]\n"
"ds_write_b128 v242, v[204:207] offset:0\n"
"v_mfma_f32_16x16x32_bf16 v[104:107], a[0:3], v[144:147], v[104:107]\n"
"v_mfma_f32_16x16x32_bf16 v[104:107], a[8:11], v[148:151], v[104:107]\n"
"v_mfma_f32_16x16x32_bf16 v[104:107], a[16:19], v[152:155], v[104:107]\n"
"s_waitcnt vmcnt(10) lgkmcnt(2)\n"
"s_barrier\n"
"v_mfma_f32_16x16x32_bf16 v[104:107], a[24:27], v[156:159], v[104:107]\n"
"ds_read_b128 v[144:147], v14 offset:16384\n"
"ds_read_b128 v[148:151], v15 offset:16384\n"
"v_mfma_f32_16x16x32_bf16 v[104:107], a[32:35], v[160:163], v[104:107]\n"
"ds_read_b128 v[152:155], v14 offset:20480\n"
"s_add_u32 m0, s75, 98304\n"
"v_mfma_f32_16x16x32_bf16 v[104:107], a[40:43], v[164:167], v[104:107]\n"
"ds_read_b128 v[156:159], v15 offset:20480\n"
"s_lshl_b32 s63, s92, 6\n"
"v_mfma_f32_16x16x32_bf16 v[104:107], a[48:51], v[168:171], v[104:107]\n"
"buffer_load_dwordx4 v18, s[24:27], s63 offen offset:0 lds\n"
"v_mfma_f32_16x16x32_bf16 v[104:107], a[56:59], v[172:175], v[104:107]\n"
"ds_write_b128 v242, v[196:199] offset:5600\n"
"v_mfma_f32_16x16x32_bf16 v[108:111], a[36:39], v[160:163], v[108:111]\n"
"v_alignbit_b32 v208, v197, v196, 16\n"
"v_alignbit_b32 v209, v198, v197, 16\n"
"v_alignbit_b32 v210, v199, v198, 16\n"
"v_mfma_f32_16x16x32_bf16 v[108:111], a[44:47], v[164:167], v[108:111]\n"
"ds_read_b128 v[160:163], v14 offset:24576\n"
"v_alignbit_b32 v211, v241, v199, 16\n"
"v_mfma_f32_16x16x32_bf16 v[108:111], a[52:55], v[168:171], v[108:111]\n"
"ds_read_b128 v[164:167], v15 offset:24576\n"
"v_mfma_f32_16x16x32_bf16 v[108:111], a[60:63], v[172:175], v[108:111]\n"
"ds_read_b128 v[168:171], v14 offset:28672\n"
"s_waitcnt lgkmcnt(7)\n"
"v_mfma_f32_16x16x32_bf16 v[100:103], a[68:71], v[144:147], v[100:103]\n"
"ds_read_b128 v[172:175], v15 offset:28672\n"
"buffer_load_dwordx4 v19, s[24:27], s63 offen offset:1024 lds\n"
"s_waitcnt lgkmcnt(7)\n"
"v_mfma_f32_16x16x32_bf16 v[100:103], a[76:79], v[148:151], v[100:103]\n"
"s_waitcnt lgkmcnt(5)\n"
"v_mfma_f32_16x16x32_bf16 v[100:103], a[84:87], v[152:155], v[100:103]\n"
"buffer_load_dwordx4 v20, s[24:27], s63 offen offset:2048 lds\n"
"ds_write_b128 v242, v[208:211] offset:1024\n"
"v_mfma_f32_16x16x32_bf16 v[100:103], a[92:95], v[156:159], v[100:103]\n"
"v_mfma_f32_16x16x32_bf16 v[96:99], a[64:67], v[144:147], v[96:99]\n"
"v_mfma_f32_16x16x32_bf16 v[96:99], a[72:75], v[148:151], v[96:99]\n"
"ds_read_b128 v[144:147], v14 offset:18432\n"
"ds_read_b128 v[148:151], v15 offset:18432\n"
"v_mfma_f32_16x16x32_bf16 v[96:99], a[80:83], v[152:155], v[96:99]\n"
"v_mfma_f32_16x16x32_bf16 v[96:99], a[88:91], v[156:159], v[96:99]\n"
"ds_read_b128 v[152:155], v14 offset:22528\n"
"s_waitcnt lgkmcnt(5)\n"
"v_mfma_f32_16x16x32_bf16 v[96:99], a[96:99], v[160:163], v[96:99]\n"
"ds_read_b128 v[156:159], v15 offset:22528\n"
"v_mfma_f32_16x16x32_bf16 v[96:99], a[104:107], v[164:167], v[96:99]\n"
"buffer_load_dwordx4 v21, s[24:27], s63 offen offset:3072 lds\n"
"v_mfma_f32_16x16x32_bf16 v[96:99], a[112:115], v[168:171], v[96:99]\n"
"s_waitcnt lgkmcnt(5)\n"
"v_mfma_f32_16x16x32_bf16 v[96:99], a[120:123], v[172:175], v[96:99]\n"
"s_waitcnt vmcnt(10)\n"
"ds_read_b64_tr_b16 a[160:161], v232 offset:32768\n"
"v_mfma_f32_16x16x32_bf16 v[100:103], a[100:103], v[160:163], v[100:103]\n"
"ds_read_b128 v[160:163], v14 offset:26624\n"
"s_add_u32 m0, s75, 114688\n"
"s_waitcnt vmcnt(8)\n"
"v_cmp_ne_u64 vcc, v[38:39], v[2:3]\n"
"s_cbranch_vccnz LslowA_m_%=\n"
"LokA_m_%=:\n"
"v_mfma_f32_16x16x32_bf16 v[100:103], a[108:111], v[164:167], v[100:103]\n"
"ds_read_b128 v[164:167], v15 offset:26624\n"
"s_lshl_b32 s63, s93, 6\n"
"v_mfma_f32_16x16x32_bf16 v[100:103], a[116:119], v[168:171], v[100:103]\n"
"ds_read_b128 v[168:171], v14 offset:30720\n"
"s_add_u32 s64, s53, 114688\n"
"v_mfma_f32_16x16x32_bf16 v[100:103], a[124:127], v[172:175], v[100:103]\n"
"ds_read_b128 v[172:175], v15 offset:30720\n"
"s_waitcnt lgkmcnt(7)\n"
"v_mfma_f32_16x16x32_bf16 v[108:111], a[68:71], v[144:147], v[108:111]\n"
"buffer_load_dwordx4 v18, s[24:27], s63 offen offset:0 lds\n"
"v_mfma_f32_16x16x32_bf16 v[108:111], a[76:79], v[148:151], v[108:111]\n"
"ds_read_b64_tr_b16 a[162:163], v232 offset:33280\n"
"s_waitcnt lgkmcnt(6)\n"
"v_mfma_f32_16x16x32_bf16 v[108:111], a[84:87], v[152:155], v[108:111]\n"
"v_max_f32 v96, 0, v96\n"
"v_mul_f32 v96, v96, v100\n"
"v_mfma_f32_16x16x32_bf16 v[108:111], a[92:95], v[156:159], v[108:111]\n"
"buffer_load_dwordx4 v19, s[24:27], s63 offen offset:1024 lds\n"
"v_mfma_f32_16x16x32_bf16 v[104:107], a[64:67], v[144:147], v[104:107]\n"
"v_max_f32 v97, 0, v97\n"
"v_mul_f32 v97, v97, v101\n"
"v_max_f32 v98, 0, v98\n"
"v_mfma_f32_16x16x32_bf16 v[104:107], a[72:75], v[148:151], v[104:107]\n"
"v_mul_f32 v98, v98, v102\n"
"v_max_f32 v99, 0, v99\n"
"v_mul_f32 v99, v99, v103\n"
"v_mfma_f32_16x16x32_bf16 v[104:107], a[80:83], v[152:155], v[104:107]\n"
"s_waitcnt lgkmcnt(1)\n"
"s_barrier\n"
"v_mfma_f32_16x16x32_bf16 v[104:107], a[88:91], v[156:159], v[104:107]\n"
"v_cvt_pk_bf16_f32 v100, v96, v97\n"
"v_cvt_pk_bf16_f32 v101, v98, v99\n"
"ds_read_b64_tr_b16 a[164:165], v232 offset:32776\n"
"v_mfma_f32_16x16x32_bf16 v[104:107], a[96:99], v[160:163], v[104:107]\n"
"ds_read_b128 a[0:3], v223 offset:3776\n"
"ds_read_b64_tr_b16 a[166:167], v232 offset:33288\n"
"ds_read_b64_tr_b16 a[168:169], v233 offset:32768\n"
"v_mfma_f32_16x16x32_bf16 v[104:107], a[104:107], v[164:167], v[104:107]\n"
"ds_read_b128 a[4:7], v222 offset:128\n"
"v_mfma_f32_16x16x32_bf16 v[104:107], a[112:115], v[168:171], v[104:107]\n"
"ds_read_b128 a[8:11], v222 offset:640\n"
"v_mfma_f32_16x16x32_bf16 v[104:107], a[120:123], v[172:175], v[104:107]\n"
"ds_read_b128 a[12:15], v223 offset:3264\n"
"buffer_load_dwordx4 v20, s[24:27], s63 offen offset:2048 lds\n"
"v_mfma_f32_16x16x32_bf16 v[108:111], a[100:103], v[160:163], v[108:111]\n"
"ds_read_b64_tr_b16 a[170:171], v233 offset:33280\n"
"ds_read_b64_tr_b16 a[172:173], v233 offset:32776\n"
"v_mfma_f32_16x16x32_bf16 v[108:111], a[108:111], v[164:167], v[108:111]\n"
"ds_read_b128 a[16:19], v222 offset:1152\n"
"ds_read_b64_tr_b16 a[174:175], v233 offset:33288\n"
"v_mfma_f32_16x16x32_bf16 v[108:111], a[116:119], v[168:171], v[108:111]\n"
"ds_read_b128 a[20:23], v223 offset:2752\n"
"v_mfma_f32_16x16x32_bf16 v[108:111], a[124:127], v[172:175], v[108:111]\n"
"Ljoin_%=:\n"
"s_waitcnt lgkmcnt(7)\n"
"v_mfma_f32_16x16x32_bf16 a[96:99], a[4:7], a[0:3], 0\n"
"buffer_load_dwordx4 v21, s[24:27], s63 offen offset:3072 lds\n"
"s_waitcnt lgkmcnt(6)\n"
"v_mfma_f32_16x16x32_bf16 a[100:103], a[8:11], a[0:3], 0\n"
"ds_read_b128 a[24:27], v222 offset:1664\n"
"s_waitcnt lgkmcnt(6)\n"
"v_mfma_f32_16x16x32_bf16 a[100:103], a[4:7], a[12:15], a[100:103]\n"
"ds_read_b128 a[28:31], v223 offset:2240\n"
"v_max_f32 v104, 0, v104\n"
"v_mfma_f32_16x16x32_bf16 a[104:107], a[8:11], a[12:15], 0\n"
"v_mul_f32 v104, v104, v108\n"
"s_waitcnt lgkmcnt(4)\n"
"v_mfma_f32_16x16x32_bf16 a[104:107], a[16:19], a[0:3], a[104:107]\n"
"v_max_f32 v105, 0, v105\n"
"v_mul_f32 v105, v105, v109\n"
"v_max_f32 v106, 0, v106\n"
"s_waitcnt lgkmcnt(2)\n"
"v_mfma_f32_16x16x32_bf16 a[104:107], a[4:7], a[20:23], a[104:107]\n"
"v_mul_f32 v106, v106, v110\n"
"v_max_f32 v107, 0, v107\n"
"v_mul_f32 v107, v107, v111\n"
"v_mfma_f32_16x16x32_bf16 a[108:111], a[16:19], a[12:15], 0\n"
"ds_read_b128 a[32:35], v222 offset:2176\n"
"v_cvt_pk_bf16_f32 v102, v104, v105\n"
"v_cvt_pk_bf16_f32 v103, v106, v107\n"
"v_mfma_f32_16x16x32_bf16 a[108:111], a[8:11], a[20:23], a[108:111]\n"
"ds_read_b128 a[36:39], v223 offset:1728\n"
"v_permlane16_swap_b32 v100, v102\n"
"s_waitcnt lgkmcnt(3)\n"
"v_mfma_f32_16x16x32_bf16 a[108:111], a[24:27], a[0:3], a[108:111]\n"
"v_permlane16_swap_b32 v101, v103\n"
"buffer_store_dwordx4 v[100:103], v1, s[40:43], s64 offen sc1\n"
"s_waitcnt lgkmcnt(2)\n"
"v_mfma_f32_16x16x32_bf16 a[108:111], a[4:7], a[28:31], a[108:111]\n"
"s_waitcnt vmcnt(9)\n"
"ds_read_b64_tr_b16 a[176:177], v232 offset:49152\n"
"ds_read_b64_tr_b16 a[178:179], v232 offset:49664\n"
"v_mfma_f32_16x16x32_bf16 a[112:115], a[24:27], a[12:15], 0\n"
"s_add_u32 m0, s75, 131072\n"
"s_lshl_b32 s63, s94, 6\n"
"ds_read_b64_tr_b16 a[180:181], v232 offset:49160\n"
"v_mfma_f32_16x16x32_bf16 a[112:115], a[16:19], a[20:23], a[112:115]\n"
"ds_read_b64_tr_b16 a[182:183], v232 offset:49672\n"
"ds_read_b64_tr_b16 a[184:185], v233 offset:49152\n"
"v_mfma_f32_16x16x32_bf16 a[112:115], a[8:11], a[28:31], a[112:115]\n"
"ds_read_b128 a[40:43], v222 offset:2688\n"
"s_waitcnt lgkmcnt(7)\n"
"v_mfma_f32_16x16x32_bf16 a[112:115], a[32:35], a[0:3], a[112:115]\n"
"ds_read_b128 a[44:47], v223 offset:1216\n"
"s_waitcnt lgkmcnt(7)\n"
"v_mfma_f32_16x16x32_bf16 a[112:115], a[4:7], a[36:39], a[112:115]\n"
"buffer_load_dwordx4 v18, s[24:27], s63 offen offset:0 lds\n"
"v_mfma_f32_16x16x32_bf16 a[116:119], a[32:35], a[12:15], 0\n"
"ds_read_b64_tr_b16 a[186:187], v233 offset:49664\n"
"ds_read_b64_tr_b16 a[188:189], v233 offset:49160\n"
"v_mfma_f32_16x16x32_bf16 a[116:119], a[24:27], a[20:23], a[116:119]\n"
"buffer_load_dwordx4 v19, s[24:27], s63 offen offset:1024 lds\n"
"v_mfma_f32_16x16x32_bf16 a[116:119], a[16:19], a[28:31], a[116:119]\n"
"ds_read_b64_tr_b16 a[190:191], v233 offset:49672\n"
"v_mfma_f32_16x16x32_bf16 a[116:119], a[8:11], a[36:39], a[116:119]\n"
"s_waitcnt lgkmcnt(4)\n"
"v_mfma_f32_16x16x32_bf16 a[116:119], a[40:43], a[0:3], a[116:119]\n"
"ds_read_b128 a[48:51], v222 offset:3200\n"
"s_waitcnt lgkmcnt(4)\n"
"v_mfma_f32_16x16x32_bf16 a[116:119], a[4:7], a[44:47], a[116:119]\n"
"ds_read_b128 a[52:55], v223 offset:704\n"
"v_mfma_f32_16x16x32_bf16 a[120:123], a[40:43], a[12:15], 0\n"
"buffer_load_dwordx4 v20, s[24:27], s63 offen offset:2048 lds\n"
"v_mfma_f32_16x16x32_bf16 a[120:123], a[32:35], a[20:23], a[120:123]\n"
"s_waitcnt vmcnt(8)\n"
"ds_read_b64_tr_b16 a[192:193], v232 offset:0\n"
"v_mfma_f32_16x16x32_bf16 a[120:123], a[24:27], a[28:31], a[120:123]\n"
"ds_read_b64_tr_b16 a[194:195], v232 offset:512\n"
"v_mfma_f32_16x16x32_bf16 a[120:123], a[16:19], a[36:39], a[120:123]\n"
"buffer_load_dwordx4 v21, s[24:27], s63 offen offset:3072 lds\n"
"v_mfma_f32_16x16x32_bf16 a[120:123], a[8:11], a[44:47], a[120:123]\n"
"ds_read_b64_tr_b16 a[196:197], v232 offset:8\n"
"s_waitcnt lgkmcnt(4)\n"
"v_mfma_f32_16x16x32_bf16 a[120:123], a[48:51], a[0:3], a[120:123]\n"
"ds_read_b128 a[56:59], v222 offset:3712\n"
"s_waitcnt lgkmcnt(4)\n"
"v_mfma_f32_16x16x32_bf16 a[120:123], a[4:7], a[52:55], a[120:123]\n"
"ds_read_b128 a[60:63], v223 offset:192\n"
"ds_read_b64_tr_b16 a[198:199], v232 offset:520\n"
"v_mfma_f32_16x16x32_bf16 a[124:127], a[48:51], a[12:15], 0\n"
"ds_read_b128 a[64:67], v223 offset:3712\n"
"ds_read_b64_tr_b16 a[200:201], v233 offset:0\n"
"v_mfma_f32_16x16x32_bf16 a[124:127], a[40:43], a[20:23], a[124:127]\n"
"ds_read_b128 a[68:71], v222 offset:64\n"
"v_mfma_f32_16x16x32_bf16 a[124:127], a[32:35], a[28:31], a[124:127]\n"
"ds_read_b128 a[72:75], v222 offset:576\n"
"v_mfma_f32_16x16x32_bf16 a[124:127], a[24:27], a[36:39], a[124:127]\n"
"ds_read_b128 a[76:79], v223 offset:3200\n"
"buffer_load_dwordx4 v[176:179], v59, s[20:23], s88 offen sc1\n"
"v_mfma_f32_16x16x32_bf16 a[124:127], a[16:19], a[44:47], a[124:127]\n"
"buffer_load_dwordx4 v[180:183], v59, s[20:23], s89 offen sc1\n"
"v_mfma_f32_16x16x32_bf16 a[124:127], a[8:11], a[52:55], a[124:127]\n"
"ds_read_b128 a[80:83], v222 offset:1088\n"
"s_waitcnt lgkmcnt(8)\n"
"v_mfma_f32_16x16x32_bf16 a[124:127], a[56:59], a[0:3], a[124:127]\n"
"ds_read_b128 a[84:87], v223 offset:2688\n"
"s_waitcnt lgkmcnt(8)\n"
"v_mfma_f32_16x16x32_bf16 a[124:127], a[4:7], a[60:63], a[124:127]\n"
"ds_read_b64_tr_b16 a[202:203], v233 offset:512\n"
"s_waitcnt lgkmcnt(5)\n"
"v_mfma_f32_16x16x32_bf16 a[96:99], a[68:71], a[64:67], a[96:99]\n"
"ds_read_b64_tr_b16 a[204:205], v233 offset:8\n"
"s_waitcnt lgkmcnt(5)\n"
"v_mfma_f32_16x16x32_bf16 a[100:103], a[72:75], a[64:67], a[100:103]\n"
"ds_read_b128 a[88:91], v222 offset:1600\n"
"s_waitcnt lgkmcnt(5)\n"
"v_mfma_f32_16x16x32_bf16 a[100:103], a[68:71], a[76:79], a[100:103]\n"
"ds_read_b128 a[92:95], v223 offset:2176\n"
"ds_read_b64_tr_b16 a[206:207], v233 offset:520\n"
"v_mfma_f32_16x16x32_bf16 a[104:107], a[72:75], a[76:79], a[104:107]\n"
"buffer_load_dwordx4 v[184:187], v59, s[20:23], s90 offen sc1\n"
"s_waitcnt lgkmcnt(6)\n"
"v_mfma_f32_16x16x32_bf16 a[104:107], a[80:83], a[64:67], a[104:107]\n"
"s_waitcnt lgkmcnt(5)\n"
"v_mfma_f32_16x16x32_bf16 a[104:107], a[68:71], a[84:87], a[104:107]\n"
"buffer_load_dwordx4 v[188:191], v59, s[20:23], s91 offen sc1\n"
"v_mfma_f32_16x16x32_bf16 a[108:111], a[80:83], a[76:79], a[108:111]\n"
"ds_read_b128 a[12:15], v222 offset:2112\n"
"v_mfma_f32_16x16x32_bf16 a[108:111], a[72:75], a[84:87], a[108:111]\n"
"ds_read_b128 a[48:51], v223 offset:1664\n"
"s_waitcnt lgkmcnt(4)\n"
"v_mfma_f32_16x16x32_bf16 a[108:111], a[88:91], a[64:67], a[108:111]\n"
"s_waitcnt lgkmcnt(3)\n"
"v_mfma_f32_16x16x32_bf16 a[108:111], a[68:71], a[92:95], a[108:111]\n"
"buffer_load_dwordx4 v[128:131], v59, s[20:23], s92 offen sc1\n"
"v_mfma_f32_16x16x32_bf16 a[112:115], a[88:91], a[76:79], a[112:115]\n"
"s_waitcnt vmcnt(10)\n"
"ds_read_b64_tr_b16 a[208:209], v232 offset:16384\n"
"v_mfma_f32_16x16x32_bf16 a[112:115], a[80:83], a[84:87], a[112:115]\n"
"ds_read_b64_tr_b16 a[210:211], v232 offset:16896\n"
"ds_read_b64_tr_b16 a[212:213], v232 offset:16392\n"
"v_mfma_f32_16x16x32_bf16 a[112:115], a[72:75], a[92:95], a[112:115]\n"
"ds_read_b128 a[20:23], v222 offset:2624\n"
"s_waitcnt lgkmcnt(5)\n"
"v_mfma_f32_16x16x32_bf16 a[112:115], a[12:15], a[64:67], a[112:115]\n"
"ds_read_b128 a[40:43], v223 offset:1152\n"
"s_waitcnt lgkmcnt(5)\n"
"v_mfma_f32_16x16x32_bf16 a[112:115], a[68:71], a[48:51], a[112:115]\n"
"buffer_load_dwordx4 v[132:135], v59, s[20:23], s93 offen sc1\n"
"v_mfma_f32_16x16x32_bf16 a[116:119], a[12:15], a[76:79], a[116:119]\n"
"s_lshl_b32 s65, s34, 16\n"
"s_and_b32 s65, s65, 0x1fff0000\n"
"s_or_b32 s65, s65, 0x20000000\n"
"v_mfma_f32_16x16x32_bf16 a[116:119], a[88:91], a[84:87], a[116:119]\n"
"buffer_load_dwordx4 v[136:139], v59, s[20:23], s94 offen sc1\n"
"v_mov_b32 v33, s65\n"
"v_mfma_f32_16x16x32_bf16 a[116:119], a[80:83], a[92:95], a[116:119]\n"
"ds_read_b64_tr_b16 a[214:215], v232 offset:16904\n"
"ds_read_b64_tr_b16 a[216:217], v233 offset:16384\n"
"v_mfma_f32_16x16x32_bf16 a[116:119], a[72:75], a[48:51], a[116:119]\n"
"ds_read_b64_tr_b16 a[218:219], v233 offset:16896\n"
"s_waitcnt lgkmcnt(4)\n"
"v_mfma_f32_16x16x32_bf16 a[116:119], a[20:23], a[64:67], a[116:119]\n"
"ds_read_b128 a[28:31], v222 offset:3136\n"
"s_waitcnt lgkmcnt(4)\n"
"v_mfma_f32_16x16x32_bf16 a[116:119], a[68:71], a[40:43], a[116:119]\n"
"ds_read_b128 a[32:35], v223 offset:640\n"
"ds_read_b64_tr_b16 a[220:221], v233 offset:16392\n"
"v_mfma_f32_16x16x32_bf16 a[120:123], a[20:23], a[76:79], a[120:123]\n"
"buffer_load_dwordx4 v[140:143], v60, s[20:23], s88 offen sc1\n"
"v_mfma_f32_16x16x32_bf16 a[120:123], a[12:15], a[84:87], a[120:123]\n"
"ds_read_b64_tr_b16 a[222:223], v233 offset:16904\n"
"v_mfma_f32_16x16x32_bf16 a[120:123], a[88:91], a[92:95], a[120:123]\n"
"v_mfma_f32_16x16x32_bf16 a[120:123], a[80:83], a[48:51], a[120:123]\n"
"buffer_load_dwordx4 v[192:195], v60, s[20:23], s89 offen sc1\n"
"v_mfma_f32_16x16x32_bf16 a[120:123], a[72:75], a[40:43], a[120:123]\n"
"s_waitcnt lgkmcnt(3)\n"
"v_mfma_f32_16x16x32_bf16 a[120:123], a[28:31], a[64:67], a[120:123]\n"
"ds_read_b128 a[24:27], v222 offset:3648\n"
"s_waitcnt lgkmcnt(3)\n"
"v_mfma_f32_16x16x32_bf16 a[120:123], a[68:71], a[32:35], a[120:123]\n"
"ds_read_b128 a[36:39], v223 offset:128\n"
"v_mfma_f32_16x16x32_bf16 a[124:127], a[28:31], a[76:79], a[124:127]\n"
"ds_read_b128 a[16:19], v223 offset:3648\n"
"v_mfma_f32_16x16x32_bf16 a[124:127], a[20:23], a[84:87], a[124:127]\n"
"ds_read_b128 a[44:47], v222 offset:0\n"
"v_mfma_f32_16x16x32_bf16 a[124:127], a[12:15], a[92:95], a[124:127]\n"
"ds_read_b128 a[8:11], v222 offset:512\n"
"s_waitcnt vmcnt(13)\n"
"buffer_store_dwordx2 v[2:3], v4, s[40:43], s54 offen offset:128 sc1\n"
"v_mfma_f32_16x16x32_bf16 a[124:127], a[88:91], a[48:51], a[124:127]\n"
"ds_read_b128 a[52:55], v223 offset:3136\n"
"buffer_load_dwordx4 v[196:199], v60, s[20:23], s90 offen sc1\n"
"v_mfma_f32_16x16x32_bf16 a[124:127], a[80:83], a[40:43], a[124:127]\n"
"v_mfma_f32_16x16x32_bf16 a[124:127], a[72:75], a[32:35], a[124:127]\n"
"ds_read_b128 a[0:3], v222 offset:1024\n"
"s_waitcnt lgkmcnt(6)\n"
"v_mfma_f32_16x16x32_bf16 a[124:127], a[24:27], a[64:67], a[124:127]\n"
"ds_read_b128 a[56:59], v223 offset:2624\n"
"s_waitcnt lgkmcnt(6)\n"
"v_mfma_f32_16x16x32_bf16 a[124:127], a[68:71], a[36:39], a[124:127]\n"
"buffer_load_dwordx4 v[200:203], v60, s[20:23], s91 offen sc1\n"
"s_waitcnt lgkmcnt(4)\n"
"v_mfma_f32_16x16x32_bf16 a[96:99], a[44:47], a[16:19], a[96:99]\n"
"s_waitcnt lgkmcnt(3)\n"
"v_mfma_f32_16x16x32_bf16 a[100:103], a[8:11], a[16:19], a[100:103]\n"
"ds_read_b128 a[4:7], v222 offset:1536\n"
"s_waitcnt lgkmcnt(3)\n"
"v_mfma_f32_16x16x32_bf16 a[100:103], a[44:47], a[52:55], a[100:103]\n"
"ds_read_b128 a[60:63], v223 offset:2112\n"
"v_mfma_f32_16x16x32_bf16 a[104:107], a[8:11], a[52:55], a[104:107]\n"
"buffer_load_dwordx4 v[204:207], v60, s[20:23], s92 offen sc1\n"
"s_waitcnt lgkmcnt(3)\n"
"v_mfma_f32_16x16x32_bf16 a[104:107], a[0:3], a[16:19], a[104:107]\n"
"s_waitcnt lgkmcnt(2)\n"
"v_mfma_f32_16x16x32_bf16 a[104:107], a[44:47], a[56:59], a[104:107]\n"
"buffer_load_dwordx4 v[208:211], v60, s[20:23], s93 offen sc1\n"
"v_mfma_f32_16x16x32_bf16 a[108:111], a[0:3], a[52:55], a[108:111]\n"
"ds_read_b128 a[76:79], v222 offset:2048\n"
"v_mfma_f32_16x16x32_bf16 a[108:111], a[8:11], a[56:59], a[108:111]\n"
"ds_read_b128 a[28:31], v223 offset:1600\n"
"s_waitcnt lgkmcnt(3)\n"
"v_mfma_f32_16x16x32_bf16 a[108:111], a[4:7], a[16:19], a[108:111]\n"
"s_waitcnt vmcnt(14)\n"
"ds_read_b64_tr_b16 a[224:225], v232 offset:32768\n"
"s_waitcnt lgkmcnt(3)\n"
"v_mfma_f32_16x16x32_bf16 a[108:111], a[44:47], a[60:63], a[108:111]\n"
"buffer_load_dwordx4 v[212:215], v60, s[20:23], s94 offen sc1\n"
"v_mfma_f32_16x16x32_bf16 a[112:115], a[4:7], a[52:55], a[112:115]\n"
"ds_read_b64_tr_b16 a[226:227], v232 offset:33280\n"
"ds_read_b64_tr_b16 a[228:229], v232 offset:32776\n"
"v_mfma_f32_16x16x32_bf16 a[112:115], a[0:3], a[56:59], a[112:115]\n"
"ds_read_b64_tr_b16 a[230:231], v232 offset:33288\n"
"ds_read_b64_tr_b16 a[232:233], v233 offset:32768\n"
"v_mfma_f32_16x16x32_bf16 a[112:115], a[8:11], a[60:63], a[112:115]\n"
"ds_read_b128 a[84:87], v222 offset:2560\n"
"s_waitcnt lgkmcnt(7)\n"
"v_mfma_f32_16x16x32_bf16 a[112:115], a[76:79], a[16:19], a[112:115]\n"
"ds_read_b128 a[20:23], v223 offset:1088\n"
"s_waitcnt lgkmcnt(7)\n"
"v_mfma_f32_16x16x32_bf16 a[112:115], a[44:47], a[28:31], a[112:115]\n"
"buffer_load_dwordx4 v[216:219], v61, s[20:23], s88 offen sc1\n"
"v_mfma_f32_16x16x32_bf16 a[116:119], a[76:79], a[52:55], a[116:119]\n"
"ds_read_b64_tr_b16 a[234:235], v233 offset:33280\n"
"ds_read_b64_tr_b16 a[236:237], v233 offset:32776\n"
"v_mfma_f32_16x16x32_bf16 a[116:119], a[4:7], a[56:59], a[116:119]\n"
"buffer_load_dwordx4 a[240:243], v61, s[20:23], s89 offen sc1\n"
"v_mfma_f32_16x16x32_bf16 a[116:119], a[0:3], a[60:63], a[116:119]\n"
"ds_read_b64_tr_b16 a[238:239], v233 offset:33288\n"
"v_mfma_f32_16x16x32_bf16 a[116:119], a[8:11], a[28:31], a[116:119]\n"
"s_waitcnt lgkmcnt(4)\n"
"v_mfma_f32_16x16x32_bf16 a[116:119], a[84:87], a[16:19], a[116:119]\n"
"ds_read_b128 a[92:95], v222 offset:3072\n"
"s_waitcnt lgkmcnt(4)\n"
"v_mfma_f32_16x16x32_bf16 a[116:119], a[44:47], a[20:23], a[116:119]\n"
"ds_read_b128 a[12:15], v223 offset:576\n"
"v_mfma_f32_16x16x32_bf16 a[120:123], a[84:87], a[52:55], a[120:123]\n"
"buffer_load_dwordx4 a[244:247], v61, s[20:23], s90 offen sc1\n"
"v_mfma_f32_16x16x32_bf16 a[120:123], a[76:79], a[56:59], a[120:123]\n"
"v_mfma_f32_16x16x32_bf16 a[120:123], a[4:7], a[60:63], a[120:123]\n"
"v_mfma_f32_16x16x32_bf16 a[120:123], a[0:3], a[28:31], a[120:123]\n"
"buffer_load_dwordx4 a[248:251], v61, s[20:23], s91 offen sc1\n"
"v_mfma_f32_16x16x32_bf16 a[120:123], a[8:11], a[20:23], a[120:123]\n"
"s_waitcnt lgkmcnt(1)\n"
"v_mfma_f32_16x16x32_bf16 a[120:123], a[92:95], a[16:19], a[120:123]\n"
"s_waitcnt lgkmcnt(0)\n"
"v_mfma_f32_16x16x32_bf16 a[120:123], a[44:47], a[12:15], a[120:123]\n"
"ds_read_b128 a[88:91], v222 offset:3584\n"
"v_mfma_f32_16x16x32_bf16 a[124:127], a[92:95], a[52:55], a[124:127]\n"
"ds_read_b128 a[48:51], v223 offset:64\n"
"v_mfma_f32_16x16x32_bf16 a[124:127], a[84:87], a[56:59], a[124:127]\n"
"ds_read_b128 a[80:83], v223 offset:3584\n"
"v_mfma_f32_16x16x32_bf16 a[124:127], a[76:79], a[60:63], a[124:127]\n"
"buffer_load_dwordx4 a[252:255], v61, s[20:23], s92 offen sc1\n"
"v_mfma_f32_16x16x32_bf16 a[124:127], a[4:7], a[28:31], a[124:127]\n"
"ds_read_b128 a[40:43], v222 offset:448\n"
"v_mfma_f32_16x16x32_bf16 a[124:127], a[0:3], a[20:23], a[124:127]\n"
"ds_read_b128 a[72:75], v223 offset:3072\n"
"v_mfma_f32_16x16x32_bf16 a[124:127], a[8:11], a[12:15], a[124:127]\n"
"s_waitcnt lgkmcnt(4)\n"
"v_mfma_f32_16x16x32_bf16 a[124:127], a[88:91], a[16:19], a[124:127]\n"
"s_waitcnt lgkmcnt(3)\n"
"v_mfma_f32_16x16x32_bf16 a[124:127], a[44:47], a[48:51], a[124:127]\n"
"ds_read_b128 a[32:35], v222 offset:960\n"
"buffer_load_dwordx4 v[112:115], v61, s[20:23], s93 offen sc1\n"
"s_waitcnt lgkmcnt(3)\n"
"v_mfma_f32_16x16x32_bf16 a[96:99], v[10:13], a[80:83], a[96:99]\n"
"ds_read_b128 a[64:67], v223 offset:2560\n"
"v_mfma_f32_16x16x32_bf16 a[96:99], v[224:227], a[80:83], a[96:99]\n"
"s_waitcnt lgkmcnt(3)\n"
"v_mfma_f32_16x16x32_bf16 a[100:103], a[40:43], a[80:83], a[100:103]\n"
"s_waitcnt lgkmcnt(2)\n"
"v_mfma_f32_16x16x32_bf16 a[100:103], v[10:13], a[72:75], a[100:103]\n"
"buffer_load_dwordx4 v[116:119], v61, s[20:23], s94 offen sc1\n"
"v_mfma_f32_16x16x32_bf16 a[100:103], v[224:227], a[72:75], a[100:103]\n"
"ds_read_b128 a[24:27], v222 offset:1472\n"
"v_mfma_f32_16x16x32_bf16 a[104:107], a[40:43], a[72:75], a[104:107]\n"
"ds_read_b128 a[68:71], v223 offset:2048\n"
"s_waitcnt lgkmcnt(3)\n"
"v_mfma_f32_16x16x32_bf16 a[104:107], a[32:35], a[80:83], a[104:107]\n"
"ds_write_b128 v244, a[96:99] offset:0\n"
"s_waitcnt lgkmcnt(3)\n"
"v_mfma_f32_16x16x32_bf16 a[104:107], v[10:13], a[64:67], a[104:107]\n"
"buffer_load_dwordx4 v[120:123], v58, s[20:23], s88 offen sc1\n"
"v_mfma_f32_16x16x32_bf16 a[104:107], v[224:227], a[64:67], a[104:107]\n"
"buffer_load_dwordx4 v[124:127], v58, s[20:23], s89 offen sc1\n"
"v_mfma_f32_16x16x32_bf16 a[108:111], a[32:35], a[72:75], a[108:111]\n"
"ds_write_b128 v244, a[100:103] offset:1024\n"
"v_mfma_f32_16x16x32_bf16 a[108:111], a[40:43], a[64:67], a[108:111]\n"
"s_waitcnt lgkmcnt(3)\n"
"v_mfma_f32_16x16x32_bf16 a[108:111], a[24:27], a[80:83], a[108:111]\n"
"s_waitcnt lgkmcnt(2)\n"
"v_mfma_f32_16x16x32_bf16 a[108:111], v[10:13], a[68:71], a[108:111]\n"
"ds_read_b128 a[36:39], v222 offset:1984\n"
"ds_read_b128 a[52:55], v223 offset:1536\n"
"v_mfma_f32_16x16x32_bf16 a[108:111], v[224:227], a[68:71], a[108:111]\n"
"buffer_load_dwordx4 v[96:99], v58, s[20:23], s90 offen sc1\n"
"v_mfma_f32_16x16x32_bf16 a[112:115], a[24:27], a[72:75], a[112:115]\n"
"v_mfma_f32_16x16x32_bf16 a[112:115], a[32:35], a[64:67], a[112:115]\n"
"ds_write_b128 v244, a[104:107] offset:2048\n"
"v_mfma_f32_16x16x32_bf16 a[112:115], a[40:43], a[68:71], a[112:115]\n"
"s_waitcnt lgkmcnt(2)\n"
"v_mfma_f32_16x16x32_bf16 a[112:115], a[36:39], a[80:83], a[112:115]\n"
"s_waitcnt lgkmcnt(1)\n"
"v_mfma_f32_16x16x32_bf16 a[112:115], v[10:13], a[52:55], a[112:115]\n"
"ds_read_b128 a[92:95], v222 offset:2496\n"
"ds_read_b128 a[56:59], v223 offset:1024\n"
"v_mfma_f32_16x16x32_bf16 a[112:115], v[224:227], a[52:55], a[112:115]\n"
"buffer_load_dwordx4 v[100:103], v58, s[20:23], s91 offen sc1\n"
"v_mfma_f32_16x16x32_bf16 a[116:119], a[36:39], a[72:75], a[116:119]\n"
"buffer_load_dwordx4 v[104:107], v58, s[20:23], s92 offen sc1\n"
"ds_write_b128 v244, a[108:111] offset:3072\n"
"v_mfma_f32_16x16x32_bf16 a[116:119], a[24:27], a[64:67], a[116:119]\n"
"v_mfma_f32_16x16x32_bf16 a[116:119], a[32:35], a[68:71], a[116:119]\n"
"v_mfma_f32_16x16x32_bf16 a[116:119], a[40:43], a[52:55], a[116:119]\n"
"s_waitcnt lgkmcnt(2)\n"
"v_mfma_f32_16x16x32_bf16 a[116:119], a[92:95], a[80:83], a[116:119]\n"
"ds_write_b128 v244, a[112:115] offset:4096\n"
"s_waitcnt lgkmcnt(2)\n"
"v_mfma_f32_16x16x32_bf16 a[116:119], v[10:13], a[56:59], a[116:119]\n"
"buffer_load_dwordx4 v[108:111], v58, s[20:23], s93 offen sc1\n"
"v_mfma_f32_16x16x32_bf16 a[116:119], v[224:227], a[56:59], a[116:119]\n"
"ds_read_b128 a[84:87], v222 offset:3008\n"
"v_mfma_f32_16x16x32_bf16 a[120:123], a[92:95], a[72:75], a[120:123]\n"
"ds_read_b128 a[60:63], v223 offset:512\n"
"v_mfma_f32_16x16x32_bf16 a[120:123], a[36:39], a[64:67], a[120:123]\n"
"buffer_load_dwordx4 v[40:43], v250, s[8:11], 0 offen offset:0\n"
"v_mfma_f32_16x16x32_bf16 a[120:123], a[24:27], a[68:71], a[120:123]\n"
"v_mfma_f32_16x16x32_bf16 a[120:123], a[32:35], a[52:55], a[120:123]\n"
"v_mfma_f32_16x16x32_bf16 a[120:123], a[40:43], a[56:59], a[120:123]\n"
"ds_write_b128 v244, a[116:119] offset:5120\n"
"s_waitcnt lgkmcnt(2)\n"
"v_mfma_f32_16x16x32_bf16 a[120:123], a[84:87], a[80:83], a[120:123]\n"
"s_waitcnt lgkmcnt(1)\n"
"v_mfma_f32_16x16x32_bf16 a[120:123], v[10:13], a[60:63], a[120:123]\n"
"v_mfma_f32_16x16x32_bf16 a[120:123], v[224:227], a[60:63], a[120:123]\n"
"ds_read_b128 a[76:79], v222 offset:3520\n"
"buffer_load_dwordx4 v[44:47], v250, s[8:11], 0 offen offset:64\n"
"v_mfma_f32_16x16x32_bf16 a[124:127], a[84:87], a[72:75], a[124:127]\n"
"ds_read_b128 a[4:7], v223 offset:0\n"
"v_mfma_f32_16x16x32_bf16 a[124:127], a[92:95], a[64:67], a[124:127]\n"
"v_mfma_f32_16x16x32_bf16 a[124:127], a[36:39], a[68:71], a[124:127]\n"
"v_mfma_f32_16x16x32_bf16 a[124:127], a[24:27], a[52:55], a[124:127]\n"
"v_mfma_f32_16x16x32_bf16 a[124:127], a[32:35], a[56:59], a[124:127]\n"
"ds_write_b128 v244, a[120:123] offset:6144\n"
"v_mfma_f32_16x16x32_bf16 a[124:127], a[40:43], a[60:63], a[124:127]\n"
"s_waitcnt lgkmcnt(2)\n"
"v_mfma_f32_16x16x32_bf16 a[124:127], a[76:79], a[80:83], a[124:127]\n"
"s_waitcnt lgkmcnt(1)\n"
"v_mfma_f32_16x16x32_bf16 a[124:127], v[10:13], a[4:7], a[124:127]\n"
"buffer_load_dwordx2 v[38:39], v63, s[20:23], 0 offen offset:128 sc1\n"
"v_mfma_f32_16x16x32_bf16 a[124:127], v[224:227], a[4:7], a[124:127]\n"
"s_waitcnt vmcnt(24)\n"
"v_mfma_f32_16x16x32_bf16 v[64:67], a[128:131], v[176:179], 0\n"
"buffer_load_dwordx4 a[0:3], v58, s[20:23], s94 offen sc1\n"
"v_add_u32 v58, s83, v58\n"
"v_add_u32 v59, s83, v59\n"
"v_mfma_f32_16x16x32_bf16 v[64:67], a[144:147], v[180:183], v[64:67]\n"
"v_add_u32 v60, s83, v60\n"
"v_add_u32 v61, s83, v61\n"
"v_mfma_f32_16x16x32_bf16 v[64:67], a[160:163], v[184:187], v[64:67]\n"
"ds_write_b128 v244, a[124:127] offset:7168\n"
"v_mfma_f32_16x16x32_bf16 v[64:67], a[176:179], v[188:191], v[64:67]\n"
"buffer_load_dword v32, v235, s[20:23], 0 offen sc1\n"
"v_mfma_f32_16x16x32_bf16 v[64:67], a[192:195], v[128:131], v[64:67]\n"
"v_mfma_f32_16x16x32_bf16 v[64:67], a[208:211], v[132:135], v[64:67]\n"
"v_mfma_f32_16x16x32_bf16 v[64:67], a[224:227], v[136:139], v[64:67]\n"
"v_mfma_f32_16x16x32_bf16 v[68:71], a[132:135], v[176:179], 0\n"
"v_mfma_f32_16x16x32_bf16 v[68:71], a[148:151], v[180:183], v[68:71]\n"
"v_mfma_f32_16x16x32_bf16 v[68:71], a[164:167], v[184:187], v[68:71]\n"
"v_mfma_f32_16x16x32_bf16 v[68:71], a[180:183], v[188:191], v[68:71]\n"
"s_waitcnt lgkmcnt(0)\n"
"s_barrier\n"
"ds_read_b128 v[144:147], v245 offset:0\n"
"ds_read_b128 v[148:151], v246 offset:0\n"
"v_mfma_f32_16x16x32_bf16 v[68:71], a[196:199], v[128:131], v[68:71]\n"
"ds_read_b128 v[152:155], v245 offset:8192\n"
"v_mfma_f32_16x16x32_bf16 v[68:71], a[212:215], v[132:135], v[68:71]\n"
"ds_read_b128 v[156:159], v246 offset:8192\n"
"v_mfma_f32_16x16x32_bf16 v[68:71], a[228:231], v[136:139], v[68:71]\n"
"ds_read_b128 v[160:163], v245 offset:2048\n"
"v_mfma_f32_16x16x32_bf16 v[72:75], a[136:139], v[176:179], 0\n"
"ds_read_b128 v[164:167], v246 offset:2048\n"
"v_mfma_f32_16x16x32_bf16 v[72:75], a[152:155], v[180:183], v[72:75]\n"
"ds_read_b128 v[168:171], v245 offset:10240\n"
"v_mfma_f32_16x16x32_bf16 v[72:75], a[168:171], v[184:187], v[72:75]\n"
"ds_read_b128 v[172:175], v246 offset:10240\n"
"v_mfma_f32_16x16x32_bf16 v[72:75], a[184:187], v[188:191], v[72:75]\n"
"v_mfma_f32_16x16x32_bf16 v[72:75], a[200:203], v[128:131], v[72:75]\n"
"ds_write_b128 v253, v[64:67] offset:0\n"
"v_mfma_f32_16x16x32_bf16 v[72:75], a[216:219], v[132:135], v[72:75]\n"
"s_waitcnt lgkmcnt(5)\n"
"v_add_f32 v144, v144, v152\n"
"v_add_f32 v145, v145, v153\n"
"v_add_f32 v146, v146, v154\n"
"v_add_f32 v147, v147, v155\n"
"v_mfma_f32_16x16x32_bf16 v[72:75], a[232:235], v[136:139], v[72:75]\n"
"v_add_f32 v148, v148, v156\n"
"v_add_f32 v149, v149, v157\n"
"v_add_f32 v150, v150, v158\n"
"v_add_f32 v151, v151, v159\n"
"v_cvt_pk_bf16_f32 v152, v144, v148\n"
"v_mfma_f32_16x16x32_bf16 v[76:79], a[140:143], v[176:179], 0\n"
"v_cvt_pk_bf16_f32 v153, v145, v149\n"
"v_cvt_pk_bf16_f32 v154, v146, v150\n"
"v_cvt_pk_bf16_f32 v155, v147, v151\n"
"s_waitcnt lgkmcnt(1)\n"
"v_add_f32 v160, v160, v168\n"
"v_mfma_f32_16x16x32_bf16 v[76:79], a[156:159], v[180:183], v[76:79]\n"
"v_add_f32 v161, v161, v169\n"
"v_add_f32 v162, v162, v170\n"
"v_add_f32 v163, v163, v171\n"
"v_add_f32 v164, v164, v172\n"
"v_add_f32 v165, v165, v173\n"
"v_mfma_f32_16x16x32_bf16 v[76:79], a[172:175], v[184:187], v[76:79]\n"
"v_add_f32 v166, v166, v174\n"
"v_add_f32 v167, v167, v175\n"
"v_cvt_pk_bf16_f32 v168, v160, v164\n"
"v_cvt_pk_bf16_f32 v169, v161, v165\n"
"v_cvt_pk_bf16_f32 v170, v162, v166\n"
"v_mfma_f32_16x16x32_bf16 v[76:79], a[188:191], v[188:191], v[76:79]\n"
"v_cvt_pk_bf16_f32 v171, v163, v167\n"
"ds_write_b128 v253, v[68:71] offset:1024\n"
"v_mfma_f32_16x16x32_bf16 v[76:79], a[204:207], v[128:131], v[76:79]\n"
"buffer_store_dwordx4 v[152:155], v1, s[20:23], s97 offen offset:0 sc1\n"
"v_mfma_f32_16x16x32_bf16 v[76:79], a[220:223], v[132:135], v[76:79]\n"
"v_mfma_f32_16x16x32_bf16 v[76:79], a[236:239], v[136:139], v[76:79]\n"
"ds_write_b128 v253, v[72:75] offset:2048\n"
"s_waitcnt vmcnt(19)\n"
"v_mfma_f32_16x16x32_bf16 v[80:83], a[128:131], v[140:143], 0\n"
"buffer_store_dwordx4 v[168:171], v1, s[20:23], s97 offen offset:1024 sc1\n"
"v_mfma_f32_16x16x32_bf16 v[80:83], a[144:147], v[192:195], v[80:83]\n"
"v_mfma_f32_16x16x32_bf16 v[80:83], a[160:163], v[196:199], v[80:83]\n"
"v_mfma_f32_16x16x32_bf16 v[80:83], a[176:179], v[200:203], v[80:83]\n"
"v_mfma_f32_16x16x32_bf16 v[80:83], a[192:195], v[204:207], v[80:83]\n"
"ds_write_b128 v253, v[76:79] offset:3072\n"
"v_mfma_f32_16x16x32_bf16 v[80:83], a[208:211], v[208:211], v[80:83]\n"
"v_mfma_f32_16x16x32_bf16 v[80:83], a[224:227], v[212:215], v[80:83]\n"
"v_mfma_f32_16x16x32_bf16 v[84:87], a[132:135], v[140:143], 0\n"
"v_mfma_f32_16x16x32_bf16 v[84:87], a[148:151], v[192:195], v[84:87]\n"
"v_mfma_f32_16x16x32_bf16 v[84:87], a[164:167], v[196:199], v[84:87]\n"
"v_mfma_f32_16x16x32_bf16 v[84:87], a[180:183], v[200:203], v[84:87]\n"
"s_waitcnt vmcnt(4)\n"
"v_cmp_ne_u64 vcc, v[38:39], v[2:3]\n"
"s_cbranch_vccnz LslowB_m_%=\n"
"LokB_m_%=:\n"
"buffer_load_dwordx4 a[4:7], v59, s[20:23], s88 offen sc1\n"
"v_mfma_f32_16x16x32_bf16 v[84:87], a[196:199], v[204:207], v[84:87]\n"
"ds_write_b128 v254, v[80:83] offset:4096\n"
"v_mfma_f32_16x16x32_bf16 v[84:87], a[212:215], v[208:211], v[84:87]\n"
"buffer_load_dwordx4 a[8:11], v59, s[20:23], s89 offen sc1\n"
"v_mfma_f32_16x16x32_bf16 v[84:87], a[228:231], v[212:215], v[84:87]\n"
"v_mfma_f32_16x16x32_bf16 v[88:91], a[136:139], v[140:143], 0\n"
"buffer_load_dwordx4 a[12:15], v59, s[20:23], s90 offen sc1\n"
"v_mfma_f32_16x16x32_bf16 v[88:91], a[152:155], v[192:195], v[88:91]\n"
"v_mfma_f32_16x16x32_bf16 v[88:91], a[168:171], v[196:199], v[88:91]\n"
"v_mfma_f32_16x16x32_bf16 v[88:91], a[184:187], v[200:203], v[88:91]\n"
"v_mfma_f32_16x16x32_bf16 v[88:91], a[200:203], v[204:207], v[88:91]\n"
"buffer_load_dwordx4 a[16:19], v59, s[20:23], s91 offen sc1\n"
"v_mfma_f32_16x16x32_bf16 v[88:91], a[216:219], v[208:211], v[88:91]\n"
"ds_write_b128 v254, v[84:87] offset:5120\n"
"v_mfma_f32_16x16x32_bf16 v[88:91], a[232:235], v[212:215], v[88:91]\n"
"v_mfma_f32_16x16x32_bf16 v[92:95], a[140:143], v[140:143], 0\n"
"buffer_load_dwordx4 a[20:23], v59, s[20:23], s92 offen sc1\n"
"v_mfma_f32_16x16x32_bf16 v[92:95], a[156:159], v[192:195], v[92:95]\n"
"v_mfma_f32_16x16x32_bf16 v[92:95], a[172:175], v[196:199], v[92:95]\n"
"v_mfma_f32_16x16x32_bf16 v[92:95], a[188:191], v[200:203], v[92:95]\n"
"v_mfma_f32_16x16x32_bf16 v[92:95], a[204:207], v[204:207], v[92:95]\n"
"buffer_load_dwordx4 a[24:27], v59, s[20:23], s93 offen sc1\n"
"v_mfma_f32_16x16x32_bf16 v[92:95], a[220:223], v[208:211], v[92:95]\n"
"ds_write_b128 v254, v[88:91] offset:6144\n"
"v_mfma_f32_16x16x32_bf16 v[92:95], a[236:239], v[212:215], v[92:95]\n"
"v_mfma_f32_16x16x32_bf16 v[64:67], a[128:131], v[216:219], 0\n"
"v_mfma_f32_16x16x32_bf16 v[64:67], a[144:147], a[240:243], v[64:67]\n"
"buffer_load_dwordx4 a[28:31], v59, s[20:23], s94 offen sc1\n"
"v_mfma_f32_16x16x32_bf16 v[64:67], a[160:163], a[244:247], v[64:67]\n"
"v_mfma_f32_16x16x32_bf16 v[64:67], a[176:179], a[248:251], v[64:67]\n"
"v_mfma_f32_16x16x32_bf16 v[64:67], a[192:195], a[252:255], v[64:67]\n"
"v_mfma_f32_16x16x32_bf16 v[64:67], a[208:211], v[112:115], v[64:67]\n"
"buffer_load_dwordx4 a[32:35], v60, s[20:23], s88 offen sc1\n"
"ds_write_b128 v254, v[92:95] offset:7168\n"
"v_mfma_f32_16x16x32_bf16 v[64:67], a[224:227], v[116:119], v[64:67]\n"
"v_mfma_f32_16x16x32_bf16 v[68:71], a[132:135], v[216:219], 0\n"
"buffer_load_dwordx4 a[36:39], v60, s[20:23], s89 offen sc1\n"
"v_mfma_f32_16x16x32_bf16 v[68:71], a[148:151], a[240:243], v[68:71]\n"
"v_mfma_f32_16x16x32_bf16 v[68:71], a[164:167], a[244:247], v[68:71]\n"
"v_mfma_f32_16x16x32_bf16 v[68:71], a[180:183], a[248:251], v[68:71]\n"
"v_mfma_f32_16x16x32_bf16 v[68:71], a[196:199], a[252:255], v[68:71]\n"
"buffer_load_dwordx4 a[40:43], v60, s[20:23], s90 offen sc1\n"
"v_mfma_f32_16x16x32_bf16 v[68:71], a[212:215], v[112:115], v[68:71]\n"
"ds_write_b128 v255, v[64:67] offset:8192\n"
"v_mfma_f32_16x16x32_bf16 v[68:71], a[228:231], v[116:119], v[68:71]\n"
"v_mfma_f32_16x16x32_bf16 v[72:75], a[136:139], v[216:219], 0\n"
"v_mfma_f32_16x16x32_bf16 v[72:75], a[152:155], a[240:243], v[72:75]\n"
"buffer_load_dwordx4 a[44:47], v60, s[20:23], s91 offen sc1\n"
"v_mfma_f32_16x16x32_bf16 v[72:75], a[168:171], a[244:247], v[72:75]\n"
"v_mfma_f32_16x16x32_bf16 v[72:75], a[184:187], a[248:251], v[72:75]\n"
"v_mfma_f32_16x16x32_bf16 v[72:75], a[200:203], a[252:255], v[72:75]\n"
"v_mfma_f32_16x16x32_bf16 v[72:75], a[216:219], v[112:115], v[72:75]\n"
"buffer_load_dwordx4 a[48:51], v60, s[20:23], s92 offen sc1\n"
"ds_write_b128 v255, v[68:71] offset:9216\n"
"v_mfma_f32_16x16x32_bf16 v[72:75], a[232:235], v[116:119], v[72:75]\n"
"v_mfma_f32_16x16x32_bf16 v[76:79], a[140:143], v[216:219], 0\n"
"buffer_load_dwordx4 v[192:195], v60, s[20:23], s93 offen sc1\n"
"v_mfma_f32_16x16x32_bf16 v[76:79], a[156:159], a[240:243], v[76:79]\n"
"v_mfma_f32_16x16x32_bf16 v[76:79], a[172:175], a[244:247], v[76:79]\n"
"v_mfma_f32_16x16x32_bf16 v[76:79], a[188:191], a[248:251], v[76:79]\n"
"v_mfma_f32_16x16x32_bf16 v[76:79], a[204:207], a[252:255], v[76:79]\n"
"v_mfma_f32_16x16x32_bf16 v[76:79], a[220:223], v[112:115], v[76:79]\n"
"v_mfma_f32_16x16x32_bf16 v[76:79], a[236:239], v[116:119], v[76:79]\n"
"ds_write_b128 v255, v[72:75] offset:10240\n"
"s_waitcnt vmcnt(16)\n"
"v_mfma_f32_16x16x32_bf16 v[80:83], a[128:131], v[120:123], 0\n"
"s_waitcnt vmcnt(13)\n"
"buffer_store_dwordx2 v[2:3], v4, s[20:23], s98 offen sc1\n"
"v_mfma_f32_16x16x32_bf16 v[80:83], a[144:147], v[124:127], v[80:83]\n"
"buffer_load_dwordx4 v[196:199], v60, s[20:23], s94 offen sc1\n"
"v_mfma_f32_16x16x32_bf16 v[80:83], a[160:163], v[96:99], v[80:83]\n"
"ds_write_b128 v255, v[76:79] offset:11264\n"
"v_mfma_f32_16x16x32_bf16 v[80:83], a[176:179], v[100:103], v[80:83]\n"
"buffer_load_dwordx4 v[200:203], v61, s[20:23], s88 offen sc1\n"
"v_mfma_f32_16x16x32_bf16 v[80:83], a[192:195], v[104:107], v[80:83]\n"
"v_mfma_f32_16x16x32_bf16 v[80:83], a[208:211], v[108:111], v[80:83]\n"
"v_mfma_f32_16x16x32_bf16 v[80:83], a[224:227], a[0:3], v[80:83]\n"
"buffer_load_dwordx4 v[204:207], v61, s[20:23], s89 offen sc1\n"
"v_mfma_f32_16x16x32_bf16 v[84:87], a[132:135], v[120:123], 0\n"
"v_mfma_f32_16x16x32_bf16 v[84:87], a[148:151], v[124:127], v[84:87]\n"
"v_mfma_f32_16x16x32_bf16 v[84:87], a[164:167], v[96:99], v[84:87]\n"
"v_mfma_f32_16x16x32_bf16 v[84:87], a[180:183], v[100:103], v[84:87]\n"
"buffer_load_dwordx4 v[208:211], v61, s[20:23], s90 offen sc1\n"
"s_waitcnt lgkmcnt(0)\n"
"s_barrier\n"
"v_mfma_f32_16x16x32_bf16 v[84:87], a[196:199], v[104:107], v[84:87]\n"
"ds_read_b128 v[144:147], v247 offset:0\n"
"ds_read_b128 v[148:151], v247 offset:4096\n"
"v_mfma_f32_16x16x32_bf16 v[84:87], a[212:215], v[108:111], v[84:87]\n"
"ds_read_b128 v[152:155], v247 offset:8192\n"
"v_mfma_f32_16x16x32_bf16 v[84:87], a[228:231], a[0:3], v[84:87]\n"
"buffer_load_dwordx4 v[212:215], v61, s[20:23], s91 offen sc1\n"
"ds_read_b128 v[156:159], v247 offset:1024\n"
"v_mfma_f32_16x16x32_bf16 v[88:91], a[136:139], v[120:123], 0\n"
"ds_read_b128 v[160:163], v247 offset:5120\n"
"v_mfma_f32_16x16x32_bf16 v[88:91], a[152:155], v[124:127], v[88:91]\n"
"ds_read_b128 v[164:167], v247 offset:9216\n"
"v_mfma_f32_16x16x32_bf16 v[88:91], a[168:171], v[96:99], v[88:91]\n"
"v_mfma_f32_16x16x32_bf16 v[88:91], a[184:187], v[100:103], v[88:91]\n"
"buffer_load_dwordx4 a[52:55], v61, s[20:23], s92 offen sc1\n"
"s_waitcnt lgkmcnt(4)\n"
"v_add_f32 v176, v144, v148\n"
"v_mfma_f32_16x16x32_bf16 v[88:91], a[200:203], v[104:107], v[88:91]\n"
"v_add_f32 v177, v145, v149\n"
"v_add_f32 v178, v146, v150\n"
"v_add_f32 v179, v147, v151\n"
"v_mfma_f32_16x16x32_bf16 v[88:91], a[216:219], v[108:111], v[88:91]\n"
"s_waitcnt lgkmcnt(3)\n"
"v_add_f32 v176, v176, v152\n"
"v_add_f32 v177, v177, v153\n"
"v_mfma_f32_16x16x32_bf16 v[88:91], a[232:235], a[0:3], v[88:91]\n"
"buffer_load_dwordx4 v[216:219], v61, s[20:23], s93 offen sc1\n"
"v_add_f32 v178, v178, v154\n"
"v_mfma_f32_16x16x32_bf16 v[92:95], a[140:143], v[120:123], 0\n"
"v_add_f32 v179, v179, v155\n"
"ds_read_b128 v[144:147], v247 offset:2048\n"
"ds_read_b128 v[148:151], v247 offset:6144\n"
"v_mfma_f32_16x16x32_bf16 v[92:95], a[156:159], v[124:127], v[92:95]\n"
"ds_read_b128 v[152:155], v247 offset:10240\n"
"v_add_f32 v176, v176, v80\n"
"v_add_f32 v177, v177, v81\n"
"v_mfma_f32_16x16x32_bf16 v[92:95], a[172:175], v[96:99], v[92:95]\n"
"buffer_load_dwordx4 a[240:243], v61, s[20:23], s94 offen sc1\n"
"v_add_f32 v178, v178, v82\n"
"v_mfma_f32_16x16x32_bf16 v[92:95], a[188:191], v[100:103], v[92:95]\n"
"v_add_f32 v179, v179, v83\n"
"s_waitcnt lgkmcnt(3)\n"
"v_add_f32 v180, v156, v160\n"
"v_mfma_f32_16x16x32_bf16 v[92:95], a[204:207], v[104:107], v[92:95]\n"
"v_add_f32 v181, v157, v161\n"
"v_add_f32 v182, v158, v162\n"
"v_add_f32 v183, v159, v163\n"
"v_mfma_f32_16x16x32_bf16 v[92:95], a[220:223], v[108:111], v[92:95]\n"
"v_add_f32 v180, v180, v164\n"
"buffer_load_dwordx4 a[244:247], v58, s[20:23], s88 offen sc1\n"
"v_mfma_f32_16x16x32_bf16 v[92:95], a[236:239], a[0:3], v[92:95]\n"
"v_add_f32 v181, v181, v165\n"
"v_add_f32 v182, v182, v166\n"
"v_add_f32 v183, v183, v167\n"
"s_waitcnt vmcnt(16)\n"
"v_mfma_f32_16x16x32_bf16 v[64:67], a[128:131], a[4:7], 0\n"
"ds_read_b128 v[156:159], v247 offset:3072\n"
"ds_read_b128 v[160:163], v247 offset:7168\n"
"v_mfma_f32_16x16x32_bf16 v[64:67], a[144:147], a[8:11], v[64:67]\n"
"ds_read_b128 v[164:167], v247 offset:11264\n"
"v_add_f32 v180, v180, v84\n"
"buffer_load_dwordx4 a[248:251], v58, s[20:23], s89 offen sc1\n"
"v_mfma_f32_16x16x32_bf16 v[64:67], a[160:163], a[12:15], v[64:67]\n"
"v_add_f32 v181, v181, v85\n"
"v_add_f32 v182, v182, v86\n"
"v_add_f32 v183, v183, v87\n"
"v_mfma_f32_16x16x32_bf16 v[64:67], a[176:179], a[16:19], v[64:67]\n"
"v_cvt_pk_bf16_f32 v24, v176, v177\n"
"v_cvt_pk_bf16_f32 v25, v178, v33\n"
"v_cvt_pk_bf16_f32 v26, v179, v180\n"
"v_mfma_f32_16x16x32_bf16 v[64:67], a[192:195], a[20:23], v[64:67]\n"
"v_cvt_pk_bf16_f32 v27, v181, v33\n"
"s_waitcnt lgkmcnt(3)\n"
"v_add_f32 v184, v144, v148\n"
"buffer_load_dwordx4 a[252:255], v58, s[20:23], s90 offen sc1\n"
"v_mfma_f32_16x16x32_bf16 v[64:67], a[208:211], a[24:27], v[64:67]\n"
"v_add_f32 v185, v145, v149\n"
"v_add_f32 v186, v146, v150\n"
"v_add_f32 v187, v147, v151\n"
"v_mfma_f32_16x16x32_bf16 v[64:67], a[224:227], a[28:31], v[64:67]\n"
"v_add_f32 v184, v184, v152\n"
"v_add_f32 v185, v185, v153\n"
"v_add_f32 v186, v186, v154\n"
"v_mfma_f32_16x16x32_bf16 v[68:71], a[132:135], a[4:7], 0\n"
"v_add_f32 v187, v187, v155\n"
"buffer_load_dwordx4 v[112:115], v58, s[20:23], s91 offen sc1\n"
"v_mfma_f32_16x16x32_bf16 v[68:71], a[148:151], a[8:11], v[68:71]\n"
"v_add_f32 v184, v184, v88\n"
"v_add_f32 v185, v185, v89\n"
"v_add_f32 v186, v186, v90\n"
"v_mfma_f32_16x16x32_bf16 v[68:71], a[164:167], a[12:15], v[68:71]\n"
"ds_write_b128 v253, v[64:67] offset:49152\n"
"v_mfma_f32_16x16x32_bf16 v[68:71], a[180:183], a[16:19], v[68:71]\n"
"buffer_load_dwordx4 v[116:119], v58, s[20:23], s92 offen sc1\n"
"v_add_f32 v187, v187, v91\n"
"v_mfma_f32_16x16x32_bf16 v[68:71], a[196:199], a[20:23], v[68:71]\n"
"v_cvt_pk_bf16_f32 v28, v182, v183\n"
"v_cvt_pk_bf16_f32 v29, v184, v33\n"
"v_cvt_pk_bf16_f32 v30, v185, v186\n"
"v_mfma_f32_16x16x32_bf16 v[68:71], a[212:215], a[24:27], v[68:71]\n"
"v_cvt_pk_bf16_f32 v31, v187, v33\n"
"s_waitcnt lgkmcnt(1)\n"
"v_add_f32 v188, v156, v160\n"
"v_mfma_f32_16x16x32_bf16 v[68:71], a[228:231], a[28:31], v[68:71]\n"
"v_add_f32 v189, v157, v161\n"
"buffer_load_dwordx4 v[120:123], v58, s[20:23], s93 offen sc1\n"
"v_mfma_f32_16x16x32_bf16 v[72:75], a[136:139], a[4:7], 0\n"
"v_add_f32 v190, v158, v162\n"
"v_add_f32 v191, v159, v163\n"
"v_add_f32 v188, v188, v164\n"
"v_mfma_f32_16x16x32_bf16 v[72:75], a[152:155], a[8:11], v[72:75]\n"
"v_add_f32 v189, v189, v165\n"
"v_add_f32 v190, v190, v166\n"
"v_add_f32 v191, v191, v167\n"
"v_mfma_f32_16x16x32_bf16 v[72:75], a[168:171], a[12:15], v[72:75]\n"
"ds_write_b128 v253, v[68:71] offset:50176\n"
"v_mfma_f32_16x16x32_bf16 v[72:75], a[184:187], a[16:19], v[72:75]\n"
"buffer_load_dwordx2 v[38:39], v252, s[20:23], 0 offen sc1\n"
"v_add_f32 v188, v188, v92\n"
"v_mfma_f32_16x16x32_bf16 v[72:75], a[200:203], a[20:23], v[72:75]\n"
"v_add_f32 v189, v189, v93\n"
"v_add_f32 v190, v190, v94\n"
"v_add_f32 v191, v191, v95\n"
"v_mfma_f32_16x16x32_bf16 v[72:75], a[216:219], a[24:27], v[72:75]\n"
"v_cvt_pk_bf16_f32 v228, v188, v189\n"
"v_cvt_pk_bf16_f32 v229, v190, v33\n"
"v_cvt_pk_bf16_f32 v230, v191, v191\n"
"v_mfma_f32_16x16x32_bf16 v[72:75], a[232:235], a[28:31], v[72:75]\n"
"v_cvt_pk_bf16_f32 v231, v191, v33\n"
"v_and_b32 v14, 0xffff0000, v32\n"
"buffer_store_dwordx4 v[24:27], v1, s[20:23], s2 offen offset:0 sc1\n"
"v_mfma_f32_16x16x32_bf16 v[76:79], a[140:143], a[4:7], 0\n"
"v_rcp_f32 v14, v14\n"
"v_lshlrev_b32 v15, 16, v32\n"
"v_mfma_f32_16x16x32_bf16 v[76:79], a[156:159], a[8:11], v[76:79]\n"
"v_mul_f32 v15, v15, v14\n"
"v_mul_f32 v16, v14, v14\n"
"v_mul_f32 v17, v14, v14\n"
"v_mfma_f32_16x16x32_bf16 v[76:79], a[172:175], a[12:15], v[76:79]\n"
"ds_write_b128 v253, v[72:75] offset:51200\n"
"v_mfma_f32_16x16x32_bf16 v[76:79], a[188:191], a[16:19], v[76:79]\n"
"buffer_store_dwordx4 v[28:31], v1, s[20:23], s2 offen offset:1024 sc1\n"
"v_mfma_f32_16x16x32_bf16 v[76:79], a[204:207], a[20:23], v[76:79]\n"
"v_lshlrev_b32 v176, 16, v40\n"
"v_fma_f32 v128, v176, v14, -v15\n"
"v_and_b32 v177, 0xffff0000, v40\n"
"v_mfma_f32_16x16x32_bf16 v[76:79], a[220:223], a[24:27], v[76:79]\n"
"v_fma_f32 v129, v177, v14, -v15\n"
"v_lshlrev_b32 v178, 16, v41\n"
"v_fma_f32 v130, v178, v14, -v15\n"
"v_mfma_f32_16x16x32_bf16 v[76:79], a[236:239], a[28:31], v[76:79]\n"
"v_and_b32 v179, 0xffff0000, v41\n"
"v_fma_f32 v131, v179, v14, -v15\n"
"buffer_store_dwordx4 v[228:231], v1, s[20:23], s2 offen offset:2048 sc1\n"
"s_waitcnt vmcnt(17)\n"
"v_mfma_f32_16x16x32_bf16 v[80:83], a[128:131], a[32:35], 0\n"
"buffer_load_dwordx4 v[124:127], v58, s[20:23], s94 offen sc1\n"
"v_lshlrev_b32 v180, 16, v42\n"
"v_mfma_f32_16x16x32_bf16 v[80:83], a[144:147], a[36:39], v[80:83]\n"
"ds_write_b128 v253, v[76:79] offset:52224\n"
"v_mfma_f32_16x16x32_bf16 v[80:83], a[160:163], a[40:43], v[80:83]\n"
"v_fma_f32 v132, v180, v14, -v15\n"
"v_and_b32 v181, 0xffff0000, v42\n"
"v_fma_f32 v133, v181, v14, -v15\n"
"v_mfma_f32_16x16x32_bf16 v[80:83], a[176:179], a[44:47], v[80:83]\n"
"v_lshlrev_b32 v182, 16, v43\n"
"v_fma_f32 v134, v182, v14, -v15\n"
"v_and_b32 v183, 0xffff0000, v43\n"
"v_mfma_f32_16x16x32_bf16 v[80:83], a[192:195], a[48:51], v[80:83]\n"
"v_fma_f32 v135, v183, v14, -v15\n"
"v_lshlrev_b32 v184, 16, v44\n"
"v_fma_f32 v136, v184, v14, -v15\n"
"v_mfma_f32_16x16x32_bf16 v[80:83], a[208:211], v[192:195], v[80:83]\n"
"v_and_b32 v185, 0xffff0000, v44\n"
"v_fma_f32 v137, v185, v14, -v15\n"
"v_lshlrev_b32 v186, 16, v45\n"
"v_mfma_f32_16x16x32_bf16 v[80:83], a[224:227], v[196:199], v[80:83]\n"
"v_fma_f32 v138, v186, v14, -v15\n"
"v_and_b32 v187, 0xffff0000, v45\n"
"v_fma_f32 v139, v187, v14, -v15\n"
"v_mfma_f32_16x16x32_bf16 v[84:87], a[132:135], a[32:35], 0\n"
"v_lshlrev_b32 v188, 16, v46\n"
"v_fma_f32 v140, v188, v14, -v15\n"
"v_and_b32 v189, 0xffff0000, v46\n"
"v_mfma_f32_16x16x32_bf16 v[84:87], a[148:151], a[36:39], v[84:87]\n"
"ds_write_b128 v254, v[80:83] offset:53248\n"
"v_mfma_f32_16x16x32_bf16 v[84:87], a[164:167], a[40:43], v[84:87]\n"
"v_fma_f32 v141, v189, v14, -v15\n"
"v_lshlrev_b32 v190, 16, v47\n"
"v_fma_f32 v142, v190, v14, -v15\n"
"v_mfma_f32_16x16x32_bf16 v[84:87], a[180:183], a[44:47], v[84:87]\n"
"v_and_b32 v191, 0xffff0000, v47\n"
"v_fma_f32 v143, v191, v14, -v15\n"
"v_mfma_f32_16x16x32_bf16 v[84:87], a[196:199], a[48:51], v[84:87]\n"
"v_mfma_f32_16x16x32_bf16 v[84:87], a[212:215], v[192:195], v[84:87]\n"
"v_mfma_f32_16x16x32_bf16 v[84:87], a[228:231], v[196:199], v[84:87]\n"
"v_mfma_f32_16x16x32_bf16 v[88:91], a[136:139], a[32:35], 0\n"
"v_mfma_f32_16x16x32_bf16 v[88:91], a[152:155], a[36:39], v[88:91]\n"
"v_mfma_f32_16x16x32_bf16 v[88:91], a[168:171], a[40:43], v[88:91]\n"
"v_mfma_f32_16x16x32_bf16 v[88:91], a[184:187], a[44:47], v[88:91]\n"
"v_mfma_f32_16x16x32_bf16 v[88:91], a[200:203], a[48:51], v[88:91]\n"
"v_mfma_f32_16x16x32_bf16 v[88:91], a[216:219], v[192:195], v[88:91]\n"
"v_mfma_f32_16x16x32_bf16 v[88:91], a[232:235], v[196:199], v[88:91]\n"
"v_mfma_f32_16x16x32_bf16 v[92:95], a[140:143], a[32:35], 0\n"
"ds_write_b128 v254, v[84:87] offset:54272\n"
"v_mfma_f32_16x16x32_bf16 v[92:95], a[156:159], a[36:39], v[92:95]\n"
"v_mfma_f32_16x16x32_bf16 v[92:95], a[172:175], a[40:43], v[92:95]\n"
"v_mfma_f32_16x16x32_bf16 v[92:95], a[188:191], a[44:47], v[92:95]\n"
"v_mfma_f32_16x16x32_bf16 v[92:95], a[204:207], a[48:51], v[92:95]\n"
"v_mfma_f32_16x16x32_bf16 v[92:95], a[220:223], v[192:195], v[92:95]\n"
"v_mfma_f32_16x16x32_bf16 v[92:95], a[236:239], v[196:199], v[92:95]\n"
"ds_write_b128 v254, v[88:91] offset:55296\n"
"s_waitcnt vmcnt(11)\n"
"v_mfma_f32_16x16x32_bf16 v[64:67], a[128:131], v[200:203], 0\n"
"s_waitcnt vmcnt(4)\n"
"v_cmp_ne_u64 vcc, v[38:39], v[2:3]\n"
"s_cbranch_vccnz LslowY_m_%=\n"
"LokY_m_%=:\n"
"v_mfma_f32_16x16x32_bf16 v[64:67], a[144:147], v[204:207], v[64:67]\n"
"buffer_load_dwordx4 v[48:51], v251, s[20:23], 0 offen sc1\n"
"buffer_load_dwordx4 v[52:55], v251, s[20:23], s82 offen sc1\n"
"v_mfma_f32_16x16x32_bf16 v[64:67], a[160:163], v[208:211], v[64:67]\n"
"ds_write_b128 v254, v[92:95] offset:56320\n"
"v_mfma_f32_16x16x32_bf16 v[64:67], a[176:179], v[212:215], v[64:67]\n"
"v_xor_b32 v252, 64, v248\n"
"v_mfma_f32_16x16x32_bf16 v[64:67], a[192:195], a[52:55], v[64:67]\n"
"v_mfma_f32_16x16x32_bf16 v[64:67], a[208:211], v[216:219], v[64:67]\n"
"v_mfma_f32_16x16x32_bf16 v[64:67], a[224:227], a[240:243], v[64:67]\n"
"v_mfma_f32_16x16x32_bf16 v[68:71], a[132:135], v[200:203], 0\n"
"v_mfma_f32_16x16x32_bf16 v[68:71], a[148:151], v[204:207], v[68:71]\n"
"v_mfma_f32_16x16x32_bf16 v[68:71], a[164:167], v[208:211], v[68:71]\n"
"v_mfma_f32_16x16x32_bf16 v[68:71], a[180:183], v[212:215], v[68:71]\n"
"s_waitcnt lgkmcnt(0)\n"
"s_barrier\n"
"v_mfma_f32_16x16x32_bf16 v[68:71], a[196:199], a[52:55], v[68:71]\n"
"ds_read_b128 v[144:147], v247 offset:49152\n"
"ds_read_b128 v[148:151], v247 offset:50176\n"
"v_mfma_f32_16x16x32_bf16 v[68:71], a[212:215], v[216:219], v[68:71]\n"
"buffer_load_dwordx4 v[6:9], v1, s[20:23], s3 offen offset:0 sc1\n"
"ds_read_b128 v[152:155], v247 offset:51200\n"
"v_mfma_f32_16x16x32_bf16 v[68:71], a[228:231], a[240:243], v[68:71]\n"
"buffer_load_dwordx4 v[10:13], v1, s[20:23], s3 offen offset:1024 sc1\n"
"ds_write_b128 v255, v[64:67] offset:57344\n"
"v_mfma_f32_16x16x32_bf16 v[72:75], a[136:139], v[200:203], 0\n"
"v_mfma_f32_16x16x32_bf16 v[72:75], a[152:155], v[204:207], v[72:75]\n"
"buffer_load_dwordx4 v[40:43], v1, s[20:23], s3 offen offset:2048 sc1\n"
"ds_read_b128 v[156:159], v247 offset:52224\n"
"v_mfma_f32_16x16x32_bf16 v[72:75], a[168:171], v[208:211], v[72:75]\n"
"ds_read_b128 v[160:163], v247 offset:53248\n"
"v_mfma_f32_16x16x32_bf16 v[72:75], a[184:187], v[212:215], v[72:75]\n"
"ds_read_b128 v[164:167], v247 offset:54272\n"
"v_mfma_f32_16x16x32_bf16 v[72:75], a[200:203], a[52:55], v[72:75]\n"
"ds_read_b128 v[168:171], v247 offset:55296\n"
"v_mfma_f32_16x16x32_bf16 v[72:75], a[216:219], v[216:219], v[72:75]\n"
"ds_read_b128 v[172:175], v247 offset:56320\n"
"v_mfma_f32_16x16x32_bf16 v[72:75], a[232:235], a[240:243], v[72:75]\n"
"v_mfma_f32_16x16x32_bf16 v[76:79], a[140:143], v[200:203], 0\n"
"ds_write_b128 v255, v[68:71] offset:58368\n"
"v_mfma_f32_16x16x32_bf16 v[76:79], a[156:159], v[204:207], v[76:79]\n"
"s_waitcnt lgkmcnt(5)\n"
"v_fmac_f32 v176, v144, v16\n"
"v_fmac_f32 v177, v145, v16\n"
"v_mfma_f32_16x16x32_bf16 v[76:79], a[172:175], v[208:211], v[76:79]\n"
"v_fmac_f32 v178, v146, v16\n"
"v_fmac_f32 v179, v147, v16\n"
"v_fmac_f32 v180, v148, v16\n"
"v_mfma_f32_16x16x32_bf16 v[76:79], a[188:191], v[212:215], v[76:79]\n"
"ds_write_b128 v255, v[72:75] offset:59392\n"
"v_mfma_f32_16x16x32_bf16 v[76:79], a[204:207], a[52:55], v[76:79]\n"
"v_fmac_f32 v181, v149, v16\n"
"v_fmac_f32 v182, v150, v16\n"
"v_fmac_f32 v183, v151, v16\n"
"v_mfma_f32_16x16x32_bf16 v[76:79], a[220:223], v[216:219], v[76:79]\n"
"v_fmac_f32 v184, v152, v16\n"
"v_fmac_f32 v185, v153, v16\n"
"v_fmac_f32 v186, v154, v16\n"
"v_mfma_f32_16x16x32_bf16 v[76:79], a[236:239], a[240:243], v[76:79]\n"
"v_fmac_f32 v187, v155, v16\n"
"v_fmac_f32 v188, v156, v16\n"
"v_fmac_f32 v189, v157, v16\n"
"s_waitcnt vmcnt(5)\n"
"v_mfma_f32_16x16x32_bf16 v[80:83], a[128:131], a[244:247], 0\n"
"v_fmac_f32 v190, v158, v16\n"
"v_fmac_f32 v191, v159, v16\n"
"s_waitcnt lgkmcnt(2)\n"
"v_fmac_f32 v176, v160, v16\n"
"v_mfma_f32_16x16x32_bf16 v[80:83], a[144:147], a[248:251], v[80:83]\n"
"ds_write_b128 v255, v[76:79] offset:60416\n"
"v_mfma_f32_16x16x32_bf16 v[80:83], a[160:163], a[252:255], v[80:83]\n"
"v_fmac_f32 v177, v161, v16\n"
"v_fmac_f32 v178, v162, v16\n"
"v_fmac_f32 v179, v163, v16\n"
"v_mfma_f32_16x16x32_bf16 v[80:83], a[176:179], v[112:115], v[80:83]\n"
"v_fmac_f32 v180, v164, v16\n"
"v_fmac_f32 v181, v165, v16\n"
"v_fmac_f32 v182, v166, v16\n"
"v_mfma_f32_16x16x32_bf16 v[80:83], a[192:195], v[116:119], v[80:83]\n"
"v_fmac_f32 v183, v167, v16\n"
"v_fmac_f32 v184, v168, v16\n"
"v_fmac_f32 v185, v169, v16\n"
"v_mfma_f32_16x16x32_bf16 v[80:83], a[208:211], v[120:123], v[80:83]\n"
"v_fmac_f32 v186, v170, v16\n"
"v_fmac_f32 v187, v171, v16\n"
"v_fmac_f32 v188, v172, v16\n"
"v_mfma_f32_16x16x32_bf16 v[80:83], a[224:227], v[124:127], v[80:83]\n"
"v_fmac_f32 v189, v173, v16\n"
"v_fmac_f32 v190, v174, v16\n"
"v_fmac_f32 v191, v175, v16\n"
"v_mfma_f32_16x16x32_bf16 v[84:87], a[132:135], a[244:247], 0\n"
"v_mfma_f32_16x16x32_bf16 v[84:87], a[148:151], a[248:251], v[84:87]\n"
"v_mfma_f32_16x16x32_bf16 v[84:87], a[164:167], a[252:255], v[84:87]\n"
"v_mfma_f32_16x16x32_bf16 v[84:87], a[180:183], v[112:115], v[84:87]\n"
"v_mfma_f32_16x16x32_bf16 v[84:87], a[196:199], v[116:119], v[84:87]\n"
"v_mfma_f32_16x16x32_bf16 v[84:87], a[212:215], v[120:123], v[84:87]\n"
"s_waitcnt vmcnt(4)\n"
"ds_write_b128 v248, v[48:51] offset:0\n"
"v_mfma_f32_16x16x32_bf16 v[84:87], a[228:231], v[124:127], v[84:87]\n"
"v_mfma_f32_16x16x32_bf16 v[88:91], a[136:139], a[244:247], 0\n"
"v_mfma_f32_16x16x32_bf16 v[88:91], a[152:155], a[248:251], v[88:91]\n"
"v_mfma_f32_16x16x32_bf16 v[88:91], a[168:171], a[252:255], v[88:91]\n"
"s_waitcnt vmcnt(3)\n"
"ds_write_b128 v252, v[52:55] offset:1024\n"
"v_mfma_f32_16x16x32_bf16 v[88:91], a[184:187], v[112:115], v[88:91]\n"
"v_mfma_f32_16x16x32_bf16 v[88:91], a[200:203], v[116:119], v[88:91]\n"
"v_mfma_f32_16x16x32_bf16 v[88:91], a[216:219], v[120:123], v[88:91]\n"
"v_mfma_f32_16x16x32_bf16 v[88:91], a[232:235], v[124:127], v[88:91]\n"
"v_mfma_f32_16x16x32_bf16 v[92:95], a[140:143], a[244:247], 0\n"
"v_mfma_f32_16x16x32_bf16 v[92:95], a[156:159], a[248:251], v[92:95]\n"
"v_mfma_f32_16x16x32_bf16 v[92:95], a[172:175], a[252:255], v[92:95]\n"
"v_mfma_f32_16x16x32_bf16 v[92:95], a[188:191], v[112:115], v[92:95]\n"
"s_waitcnt lgkmcnt(0)\n"
"s_barrier\n"
"v_mfma_f32_16x16x32_bf16 v[92:95], a[204:207], v[116:119], v[92:95]\n"
"ds_read_b64_tr_b16 v[48:49], v249 offset:0\n"
"ds_read_b64_tr_b16 v[50:51], v249 offset:512\n"
"ds_read_b64_tr_b16 v[52:53], v249 offset:4096\n"
"v_mfma_f32_16x16x32_bf16 v[92:95], a[220:223], v[120:123], v[92:95]\n"
"ds_read_b64_tr_b16 v[54:55], v249 offset:4608\n"
"ds_read_b128 v[144:147], v247 offset:57344\n"
"ds_read_b128 v[148:151], v247 offset:58368\n"
"v_mfma_f32_16x16x32_bf16 v[92:95], a[236:239], v[124:127], v[92:95]\n"
"s_waitcnt vmcnt(0)\n"
"v_xor_b32 v22, v7, v33\n"
"v_bitop3_b32 v22, v22, v9, v33 bitop3:0xf6\n"
"v_bitop3_b32 v22, v22, v11, v33 bitop3:0xf6\n"
"v_bitop3_b32 v22, v22, v13, v33 bitop3:0xf6\n"
"v_bitop3_b32 v22, v22, v41, v33 bitop3:0xf6\n"
"v_bitop3_b32 v22, v22, v43, v33 bitop3:0xf6\n"
"v_cmp_lt_u32 vcc, 0xffff, v22\n"
"s_cbranch_vccnz LslowP_m_%=\n"
"LokP_m_%=:\n"
"v_lshlrev_b32 v64, 16, v6\n"
"v_and_b32 v65, 0xffff0000, v6\n"
"v_lshlrev_b32 v66, 16, v7\n"
"v_lshlrev_b32 v67, 16, v8\n"
"v_and_b32 v68, 0xffff0000, v8\n"
"v_lshlrev_b32 v69, 16, v9\n"
"v_lshlrev_b32 v70, 16, v10\n"
"v_and_b32 v71, 0xffff0000, v10\n"
"v_lshlrev_b32 v72, 16, v11\n"
"v_lshlrev_b32 v73, 16, v12\n"
"v_and_b32 v74, 0xffff0000, v12\n"
"v_lshlrev_b32 v75, 16, v13\n"
"v_lshlrev_b32 v76, 16, v40\n"
"v_and_b32 v77, 0xffff0000, v40\n"
"v_lshlrev_b32 v78, 16, v41\n"
"v_lshlrev_b32 v79, 16, v42\n"
"ds_read_b128 v[152:155], v247 offset:59392\n"
"ds_read_b128 v[156:159], v247 offset:60416\n"
"s_waitcnt lgkmcnt(4)\n"
"v_lshlrev_b32 v160, 16, v48\n"
"v_and_b32 v161, 0xffff0000, v48\n"
"v_lshlrev_b32 v162, 16, v49\n"
"v_and_b32 v163, 0xffff0000, v49\n"
"v_lshlrev_b32 v164, 16, v50\n"
"v_and_b32 v165, 0xffff0000, v50\n"
"v_lshlrev_b32 v166, 16, v51\n"
"v_and_b32 v167, 0xffff0000, v51\n"
"v_lshlrev_b32 v168, 16, v52\n"
"v_and_b32 v169, 0xffff0000, v52\n"
"v_lshlrev_b32 v170, 16, v53\n"
"v_and_b32 v171, 0xffff0000, v53\n"
"v_lshlrev_b32 v172, 16, v54\n"
"v_and_b32 v173, 0xffff0000, v54\n"
"v_lshlrev_b32 v174, 16, v55\n"
"v_and_b32 v175, 0xffff0000, v55\n"
"v_pk_fma_f32 v[176:177], v[160:161], v[128:129], v[176:177]\n"
"v_pk_fma_f32 v[178:179], v[162:163], v[130:131], v[178:179]\n"
"v_pk_fma_f32 v[180:181], v[164:165], v[132:133], v[180:181]\n"
"v_pk_fma_f32 v[182:183], v[166:167], v[134:135], v[182:183]\n"
"v_pk_fma_f32 v[184:185], v[168:169], v[136:137], v[184:185]\n"
"v_pk_fma_f32 v[186:187], v[170:171], v[138:139], v[186:187]\n"
"v_pk_fma_f32 v[188:189], v[172:173], v[140:141], v[188:189]\n"
"v_pk_fma_f32 v[190:191], v[174:175], v[142:143], v[190:191]\n"
"s_waitcnt lgkmcnt(2)\n"
"v_pk_add_f32 v[144:145], v[144:145], v[80:81]\n"
"v_pk_add_f32 v[146:147], v[146:147], v[82:83]\n"
"v_pk_add_f32 v[148:149], v[148:149], v[84:85]\n"
"v_pk_add_f32 v[150:151], v[150:151], v[86:87]\n"
"s_waitcnt lgkmcnt(1)\n"
"v_pk_add_f32 v[152:153], v[152:153], v[88:89]\n"
"v_pk_add_f32 v[154:155], v[154:155], v[90:91]\n"
"s_waitcnt lgkmcnt(0)\n"
"v_pk_add_f32 v[156:157], v[156:157], v[92:93]\n"
"v_pk_add_f32 v[158:159], v[158:159], v[94:95]\n"
"v_pk_add_f32 v[144:145], v[144:145], v[64:65]\n"
"v_pk_add_f32 v[146:147], v[146:147], v[66:67]\n"
"v_pk_add_f32 v[148:149], v[148:149], v[68:69]\n"
"v_pk_add_f32 v[150:151], v[150:151], v[70:71]\n"
"v_pk_fma_f32 v[176:177], v[144:145], v[16:17], v[176:177]\n"
"v_pk_fma_f32 v[178:179], v[146:147], v[16:17], v[178:179]\n"
"v_pk_fma_f32 v[180:181], v[148:149], v[16:17], v[180:181]\n"
"v_pk_fma_f32 v[182:183], v[150:151], v[16:17], v[182:183]\n"
"v_cvt_pk_bf16_f32 v128, v176, v177\n"
"v_cvt_pk_bf16_f32 v129, v178, v179\n"
"v_cvt_pk_bf16_f32 v130, v180, v181\n"
"v_cvt_pk_bf16_f32 v131, v182, v183\n"
"buffer_store_dwordx4 v[128:131], v250, s[36:39], 0 offen offset:0\n"
"v_pk_add_f32 v[152:153], v[152:153], v[72:73]\n"
"v_pk_add_f32 v[154:155], v[154:155], v[74:75]\n"
"v_pk_add_f32 v[156:157], v[156:157], v[76:77]\n"
"v_pk_add_f32 v[158:159], v[158:159], v[78:79]\n"
"v_pk_fma_f32 v[184:185], v[152:153], v[16:17], v[184:185]\n"
"v_pk_fma_f32 v[186:187], v[154:155], v[16:17], v[186:187]\n"
"v_pk_fma_f32 v[188:189], v[156:157], v[16:17], v[188:189]\n"
"v_pk_fma_f32 v[190:191], v[158:159], v[16:17], v[190:191]\n"
"v_cvt_pk_bf16_f32 v132, v184, v185\n"
"v_cvt_pk_bf16_f32 v133, v186, v187\n"
"v_cvt_pk_bf16_f32 v134, v188, v189\n"
"v_cvt_pk_bf16_f32 v135, v190, v191\n"
"buffer_store_dwordx4 v[132:135], v250, s[36:39], 0 offen offset:64\n"
"s_endpgm\n"
"LslowS_m_%=:\n"
"s_mov_b32 s99, 0\n"
"LslS_m_%=:\n"
"buffer_load_dwordx4 v[40:43], v236, s[20:23], 0 offen sc1\n"
"buffer_load_dwordx4 v[44:47], v237, s[20:23], 0 offen sc1\n"
"buffer_load_dwordx4 v[48:51], v238, s[20:23], 0 offen sc1\n"
"buffer_load_dwordx4 v[52:55], v239, s[20:23], 0 offen sc1\n"
"s_waitcnt vmcnt(0)\n"
"v_xor_b32 v22, v41, v2\n"
"v_bitop3_b32 v22, v22, v43, v2 bitop3:0xf6\n"
"v_bitop3_b32 v22, v22, v45, v2 bitop3:0xf6\n"
"v_bitop3_b32 v22, v22, v47, v2 bitop3:0xf6\n"
"v_bitop3_b32 v22, v22, v49, v2 bitop3:0xf6\n"
"v_bitop3_b32 v22, v22, v51, v2 bitop3:0xf6\n"
"v_bitop3_b32 v22, v22, v53, v2 bitop3:0xf6\n"
"v_bitop3_b32 v22, v22, v55, v2 bitop3:0xf6\n"
"v_cmp_ne_u32 vcc, 0, v22\n"
"s_cbranch_vccz LokS_m_%=\n"
"s_sleep 1\n"
"s_add_u32 s99, s99, 1\n"
"s_cmp_lt_u32 s99, 0x8000\n"
"s_cbranch_scc1 LslS_m_%=\n"
"s_branch LokS_m_%=\n"
"LslowK_m_%=:\n"
"s_mov_b32 s99, 0\n"
"LslK_m_%=:\n"
"buffer_load_dwordx2 v[38:39], v62, s[20:23], 0 offen offset:0 sc1\n"
"s_waitcnt vmcnt(0)\n"
"v_cmp_ne_u64 vcc, v[38:39], v[2:3]\n"
"s_cbranch_vccz LokK_m_%=\n"
"s_sleep 1\n"
"s_add_u32 s99, s99, 1\n"
"s_cmp_lt_u32 s99, 0x8000\n"
"s_cbranch_scc1 LslK_m_%=\n"
"s_branch LokK_m_%=\n"
"LslowA_m_%=:\n"
"s_mov_b32 s99, 0\n"
"LslA_m_%=:\n"
"buffer_load_dwordx2 v[38:39], v63, s[20:23], 0 offen offset:0 sc1\n"
"s_waitcnt vmcnt(0)\n"
"v_cmp_ne_u64 vcc, v[38:39], v[2:3]\n"
"s_cbranch_vccz LokA_m_%=\n"
"s_sleep 1\n"
"s_add_u32 s99, s99, 1\n"
"s_cmp_lt_u32 s99, 0x8000\n"
"s_cbranch_scc1 LslA_m_%=\n"
"s_branch LokA_m_%=\n"
"LslowB_m_%=:\n"
"s_mov_b32 s99, 0\n"
"LslB_m_%=:\n"
"buffer_load_dwordx2 v[38:39], v63, s[20:23], 0 offen offset:128 sc1\n"
"s_waitcnt vmcnt(0)\n"
"v_cmp_ne_u64 vcc, v[38:39], v[2:3]\n"
"s_cbranch_vccz LokB_m_%=\n"
"s_sleep 1\n"
"s_add_u32 s99, s99, 1\n"
"s_cmp_lt_u32 s99, 0x8000\n"
"s_cbranch_scc1 LslB_m_%=\n"
"s_branch LokB_m_%=\n"
"LslowY_m_%=:\n"
"s_mov_b32 s99, 0\n"
"LslY_m_%=:\n"
"buffer_load_dwordx2 v[38:39], v252, s[20:23], 0 offen sc1\n"
"s_waitcnt vmcnt(0)\n"
"v_cmp_ne_u64 vcc, v[38:39], v[2:3]\n"
"s_cbranch_vccz LokY_m_%=\n"
"s_sleep 1\n"
"s_add_u32 s99, s99, 1\n"
"s_cmp_lt_u32 s99, 0x8000\n"
"s_cbranch_scc1 LslY_m_%=\n"
"s_branch LokY_m_%=\n"
"LslowP_m_%=:\n"
"s_mov_b32 s99, 0\n"
"LslP_m_%=:\n"
"buffer_load_dwordx4 v[6:9], v1, s[20:23], s3 offen offset:0 sc1\n"
"buffer_load_dwordx4 v[10:13], v1, s[20:23], s3 offen offset:1024 sc1\n"
"buffer_load_dwordx4 v[40:43], v1, s[20:23], s3 offen offset:2048 sc1\n"
"s_waitcnt vmcnt(0)\n"
"v_xor_b32 v22, v7, v33\n"
"v_bitop3_b32 v22, v22, v9, v33 bitop3:0xf6\n"
"v_bitop3_b32 v22, v22, v11, v33 bitop3:0xf6\n"
"v_bitop3_b32 v22, v22, v13, v33 bitop3:0xf6\n"
"v_bitop3_b32 v22, v22, v41, v33 bitop3:0xf6\n"
"v_bitop3_b32 v22, v22, v43, v33 bitop3:0xf6\n"
"v_cmp_lt_u32 vcc, 0xffff, v22\n"
"s_cbranch_vccz LokP_m_%=\n"
"s_sleep 1\n"
"s_add_u32 s99, s99, 1\n"
"s_cmp_lt_u32 s99, 0x8000\n"
"s_cbranch_scc1 LslP_m_%=\n"
"s_branch LokP_m_%=\n"
"Lhelp_%=:\n"
"s_load_dwordx8 s[48:55], %1, 0x80\n"
"s_load_dwordx4 s[44:47], %1, 0xa0\n"
"s_lshl_b32 s63, s80, 2\n"
"s_add_u32 s63, s63, s81\n"
"s_lshr_b32 s76, s63, 1\n"
"s_and_b32 s63, s63, 1\n"
"s_lshl_b32 s63, s63, 2\n"
"s_add_u32 s77, s63, s61\n"
"v_and_b32 v8, 7, v0\n"
"v_lshrrev_b32 v9, 3, v0\n"
"s_lshl_b32 s63, s76, 17\n"
"s_lshl_b32 s64, s77, 7\n"
"s_add_u32 s63, s63, s64\n"
"v_lshlrev_b32 v10, 4, v8\n"
"v_lshl_add_u32 v10, v9, 14, v10\n"
"v_add_u32 v5, s63, v10\n"
"v_subrev_u32 v6, 3072, v5\n"
"v_min_u32 v6, 0x7fffffff, v6\n"
"s_lshl_b32 s64, s77, 9\n"
"v_lshl_add_u32 v7, v8, 6, s64\n"
"s_mov_b32 s66, 4096\n"
"s_mov_b32 s67, 8192\n"
"s_mov_b32 s74, 12288\n"
"s_waitcnt lgkmcnt(0)\n"
"buffer_load_dwordx4 v[176:179], v54, s[8:11], 0 offen\n"
"buffer_load_dwordx4 v[180:183], v54, s[8:11], 0 offen offset:512\n"
"buffer_load_dwordx4 v[236:239], v7, s[44:47], 0 offen offset:0\n"
"buffer_load_dwordx4 v[240:243], v7, s[44:47], 0 offen offset:16\n"
"buffer_load_dwordx4 v[244:247], v7, s[44:47], 0 offen offset:32\n"
"buffer_load_dwordx4 v[248:251], v7, s[44:47], 0 offen offset:48\n"
"buffer_load_dwordx4 v[64:67], v6, s[48:51], 0 offen offset:0\n"
"buffer_load_dwordx4 v[68:71], v6, s[48:51], 0 offen offset:1024\n"
"buffer_load_dwordx4 v[72:75], v6, s[48:51], 0 offen offset:2048\n"
"buffer_load_dwordx4 v[76:79], v5, s[48:51], 0 offen offset:0\n"
"buffer_load_dwordx4 v[80:83], v5, s[52:55], 0 offen offset:0\n"
"s_lshl_b32 s60, s62, 2\n"
"s_load_dword s60, s[34:35], s60\n"
"s_load_dwordx2 s[34:35], s[20:21], 0x0\n"
"v_xor_b32 v10, v8, v9\n"
"v_lshlrev_b32 v10, 4, v10\n"
"s_lshl_b32 s64, s61, 14\n"
"v_lshl_add_u32 v206, v8, 10, v10\n"
"v_add_u32 v206, s64, v206\n"
"v_lshl_add_u32 v207, v9, 10, v10\n"
"v_add_u32 v207, s64, v207\n"
"s_lshl_b32 s63, s77, 18\n"
"s_lshl_b32 s64, s76, 8\n"
"s_add_u32 s63, s63, s64\n"
"v_lshlrev_b32 v10, 4, v8\n"
"v_lshl_add_u32 v10, v9, 15, v10\n"
"v_add_u32 v252, s63, v10\n"
"buffer_load_dwordx4 v[84:87], v5, s[48:51], 0 offen offset:1024\n"
"buffer_load_dwordx4 v[88:91], v5, s[52:55], 0 offen offset:1024\n"
"buffer_load_dwordx4 v[92:95], v5, s[48:51], 0 offen offset:2048\n"
"buffer_load_dwordx4 v[96:99], v5, s[52:55], 0 offen offset:2048\n"
"v_cmp_eq_u32 vcc, 0, v0\n"
"v_bfrev_b32 v204, 1\n"
"v_and_b32 v205, 31, v0\n"
"v_cndmask_b32 v4, v204, v0, vcc\n"
"v_cmp_eq_u32 vcc, 0, v205\n"
"v_lshrrev_b32 v205, 1, v0\n"
"s_waitcnt lgkmcnt(0)\n"
"v_cndmask_b32 v205, v204, v205, vcc\n"
"s_mul_i32 s34, s34, 1664525\n"
"s_add_u32 s34, s34, 1013904223\n"
"s_mul_i32 s35, s35, 22695477\n"
"s_add_u32 s35, s35, 1\n"
"v_mov_b32 v2, s34\n"
"v_mov_b32 v3, s35\n"
"v_mov_b32 v203, s34\n"
"s_waitcnt vmcnt(14)\n"
"v_lshlrev_b32 v184, 16, v176\n"
"v_and_b32 v185, 0xffff0000, v176\n"
"v_lshlrev_b32 v186, 16, v177\n"
"v_and_b32 v187, 0xffff0000, v177\n"
"v_lshlrev_b32 v188, 16, v178\n"
"v_and_b32 v189, 0xffff0000, v178\n"
"v_lshlrev_b32 v190, 16, v179\n"
"v_and_b32 v191, 0xffff0000, v179\n"
"s_waitcnt vmcnt(13)\n"
"v_lshlrev_b32 v192, 16, v180\n"
"v_and_b32 v193, 0xffff0000, v180\n"
"v_lshlrev_b32 v194, 16, v181\n"
"v_and_b32 v195, 0xffff0000, v181\n"
"v_lshlrev_b32 v196, 16, v182\n"
"v_and_b32 v197, 0xffff0000, v182\n"
"v_lshlrev_b32 v198, 16, v183\n"
"v_and_b32 v199, 0xffff0000, v183\n"
"v_pk_add_f32 v[176:177], v[184:185], v[186:187]\n"
"v_pk_add_f32 v[178:179], v[188:189], v[190:191]\n"
"v_pk_add_f32 v[180:181], v[192:193], v[194:195]\n"
"v_pk_add_f32 v[182:183], v[196:197], v[198:199]\n"
"v_pk_add_f32 v[176:177], v[176:177], v[178:179]\n"
"v_pk_add_f32 v[180:181], v[180:181], v[182:183]\n"
"v_pk_add_f32 v[176:177], v[176:177], v[180:181]\n"
"v_add_f32 v200, v176, v177\n"
"s_nop 1\n"
"v_add_f32_dpp v200, v200, v200 quad_perm:[1,0,3,2] row_mask:0xf bank_mask:0xf\n"
"s_nop 1\n"
"v_add_f32_dpp v200, v200, v200 quad_perm:[2,3,0,1] row_mask:0xf bank_mask:0xf\n"
"s_nop 1\n"
"v_add_f32_dpp v200, v200, v200 row_half_mirror row_mask:0xf bank_mask:0xf\n"
"s_nop 1\n"
"v_add_f32_dpp v200, v200, v200 row_mirror row_mask:0xf bank_mask:0xf\n"
"v_mov_b32 v201, v200\n"
"s_nop 1\n"
"v_permlane16_swap_b32 v200, v201\n"
"v_add_f32 v200, v200, v201\n"
"v_mul_f32 v200, 0x3b000000, v200\n"
"v_pk_add_f32 v[184:185], v[184:185], v[200:201] op_sel_hi:[1,0] neg_lo:[0,1] neg_hi:[0,1]\n"
"v_pk_add_f32 v[186:187], v[186:187], v[200:201] op_sel_hi:[1,0] neg_lo:[0,1] neg_hi:[0,1]\n"
"v_pk_add_f32 v[188:189], v[188:189], v[200:201] op_sel_hi:[1,0] neg_lo:[0,1] neg_hi:[0,1]\n"
"v_pk_add_f32 v[190:191], v[190:191], v[200:201] op_sel_hi:[1,0] neg_lo:[0,1] neg_hi:[0,1]\n"
"v_pk_add_f32 v[192:193], v[192:193], v[200:201] op_sel_hi:[1,0] neg_lo:[0,1] neg_hi:[0,1]\n"
"v_pk_add_f32 v[194:195], v[194:195], v[200:201] op_sel_hi:[1,0] neg_lo:[0,1] neg_hi:[0,1]\n"
"v_pk_add_f32 v[196:197], v[196:197], v[200:201] op_sel_hi:[1,0] neg_lo:[0,1] neg_hi:[0,1]\n"
"v_pk_add_f32 v[198:199], v[198:199], v[200:201] op_sel_hi:[1,0] neg_lo:[0,1] neg_hi:[0,1]\n"
"v_pk_mul_f32 v[176:177], v[184:185], v[184:185]\n"
"v_pk_mul_f32 v[178:179], v[186:187], v[186:187]\n"
"v_pk_fma_f32 v[176:177], v[188:189], v[188:189], v[176:177]\n"
"v_pk_fma_f32 v[178:179], v[190:191], v[190:191], v[178:179]\n"
"v_pk_fma_f32 v[176:177], v[192:193], v[192:193], v[176:177]\n"
"v_pk_fma_f32 v[178:179], v[194:195], v[194:195], v[178:179]\n"
"v_pk_fma_f32 v[176:177], v[196:197], v[196:197], v[176:177]\n"
"v_pk_fma_f32 v[178:179], v[198:199], v[198:199], v[178:179]\n"
"v_pk_add_f32 v[176:177], v[176:177], v[178:179]\n"
"v_add_f32 v176, v176, v177\n"
"s_nop 1\n"
"v_add_f32_dpp v176, v176, v176 quad_perm:[1,0,3,2] row_mask:0xf bank_mask:0xf\n"
"s_nop 1\n"
"v_add_f32_dpp v176, v176, v176 quad_perm:[2,3,0,1] row_mask:0xf bank_mask:0xf\n"
"s_nop 1\n"
"v_add_f32_dpp v176, v176, v176 row_half_mirror row_mask:0xf bank_mask:0xf\n"
"s_nop 1\n"
"v_add_f32_dpp v176, v176, v176 row_mirror row_mask:0xf bank_mask:0xf\n"
"v_mov_b32 v177, v176\n"
"s_nop 1\n"
"v_permlane16_swap_b32 v176, v177\n"
"v_add_f32 v176, v176, v177\n"
"v_mul_f32 v176, 0x3b000000, v176\n"
"v_cvt_pk_bf16_f32 v176, v176, v176\n"
"v_lshlrev_b32 v176, 16, v176\n"
"v_add_f32 v176, 0x3727c5ac, v176\n"
"v_cvt_pk_bf16_f32 v176, v176, v176\n"
"v_lshlrev_b32 v176, 16, v176\n"
"v_sqrt_f32 v176, v176\n"
"s_nop 0\n"
"v_cvt_pk_bf16_f32 v202, v200, v176\n"
"buffer_store_dwordx2 v[202:203], v205, s[20:23], s99 offen sc1\n"
"buffer_load_dwordx4 v[100:103], v5, s[48:51], 0 offen offset:3072\n"
"buffer_load_dwordx4 v[104:107], v5, s[52:55], 0 offen offset:3072\n"
"s_waitcnt vmcnt(15)\n"
"v_lshlrev_b32 v208, 16, v236\n"
"v_lshlrev_b32 v209, 16, v238\n"
"v_and_b32 v210, 0xffff0000, v236\n"
"v_and_b32 v211, 0xffff0000, v238\n"
"v_lshlrev_b32 v212, 16, v237\n"
"v_lshlrev_b32 v213, 16, v239\n"
"v_and_b32 v214, 0xffff0000, v237\n"
"v_and_b32 v215, 0xffff0000, v239\n"
"s_waitcnt vmcnt(14)\n"
"v_lshlrev_b32 v216, 16, v240\n"
"v_lshlrev_b32 v217, 16, v242\n"
"v_and_b32 v218, 0xffff0000, v240\n"
"v_and_b32 v219, 0xffff0000, v242\n"
"v_lshlrev_b32 v220, 16, v241\n"
"v_lshlrev_b32 v221, 16, v243\n"
"v_and_b32 v222, 0xffff0000, v241\n"
"v_and_b32 v223, 0xffff0000, v243\n"
"s_waitcnt vmcnt(13)\n"
"v_lshlrev_b32 v224, 16, v244\n"
"v_lshlrev_b32 v225, 16, v246\n"
"v_and_b32 v226, 0xffff0000, v244\n"
"v_and_b32 v227, 0xffff0000, v246\n"
"v_lshlrev_b32 v228, 16, v245\n"
"v_lshlrev_b32 v229, 16, v247\n"
"v_and_b32 v230, 0xffff0000, v245\n"
"v_and_b32 v231, 0xffff0000, v247\n"
"s_waitcnt vmcnt(12)\n"
"v_lshlrev_b32 v232, 16, v248\n"
"v_lshlrev_b32 v233, 16, v250\n"
"v_and_b32 v234, 0xffff0000, v248\n"
"v_and_b32 v235, 0xffff0000, v250\n"
"v_lshlrev_b32 v176, 16, v249\n"
"v_lshlrev_b32 v177, 16, v251\n"
"v_and_b32 v178, 0xffff0000, v249\n"
"v_and_b32 v179, 0xffff0000, v251\n"
"s_waitcnt vmcnt(11)\n"
"v_lshlrev_b32 v8, 16, v64\n"
"v_and_b32 v9, 0xffff0000, v64\n"
"v_lshlrev_b32 v10, 16, v65\n"
"v_and_b32 v11, 0xffff0000, v65\n"
"v_lshlrev_b32 v12, 16, v66\n"
"v_and_b32 v13, 0xffff0000, v66\n"
"v_lshlrev_b32 v14, 16, v67\n"
"v_and_b32 v15, 0xffff0000, v67\n"
"buffer_load_dwordx4 v[108:111], v5, s[48:51], s66 offen offset:0\n"
"s_waitcnt vmcnt(11)\n"
"v_lshlrev_b32 v16, 16, v68\n"
"v_and_b32 v17, 0xffff0000, v68\n"
"v_lshlrev_b32 v18, 16, v69\n"
"v_and_b32 v19, 0xffff0000, v69\n"
"v_lshlrev_b32 v20, 16, v70\n"
"v_and_b32 v21, 0xffff0000, v70\n"
"v_lshlrev_b32 v22, 16, v71\n"
"v_and_b32 v23, 0xffff0000, v71\n"
"buffer_load_dwordx4 v[112:115], v5, s[52:55], s66 offen offset:0\n"
"s_waitcnt vmcnt(11)\n"
"v_lshlrev_b32 v24, 16, v72\n"
"v_and_b32 v25, 0xffff0000, v72\n"
"v_lshlrev_b32 v26, 16, v73\n"
"v_and_b32 v27, 0xffff0000, v73\n"
"v_lshlrev_b32 v28, 16, v74\n"
"v_and_b32 v29, 0xffff0000, v74\n"
"v_lshlrev_b32 v30, 16, v75\n"
"v_and_b32 v31, 0xffff0000, v75\n"
"buffer_load_dwordx4 v[116:119], v5, s[48:51], s66 offen offset:1024\n"
"s_waitcnt vmcnt(11)\n"
"v_lshlrev_b32 v32, 16, v76\n"
"v_and_b32 v33, 0xffff0000, v76\n"
"v_lshlrev_b32 v34, 16, v77\n"
"v_and_b32 v35, 0xffff0000, v77\n"
"v_lshlrev_b32 v36, 16, v78\n"
"v_and_b32 v37, 0xffff0000, v78\n"
"v_lshlrev_b32 v38, 16, v79\n"
"v_and_b32 v39, 0xffff0000, v79\n"
"buffer_load_dwordx4 v[120:123], v5, s[52:55], s66 offen offset:1024\n"
"v_pk_mul_f32 v[48:49], v[8:9], v[214:215]\n"
"v_pk_mul_f32 v[50:51], v[10:11], v[222:223]\n"
"v_pk_mul_f32 v[52:53], v[12:13], v[230:231]\n"
"v_pk_mul_f32 v[54:55], v[14:15], v[178:179]\n"
"v_pk_fma_f32 v[48:49], v[16:17], v[212:213], v[48:49]\n"
"v_pk_fma_f32 v[50:51], v[18:19], v[220:221], v[50:51]\n"
"v_pk_fma_f32 v[52:53], v[20:21], v[228:229], v[52:53]\n"
"v_pk_fma_f32 v[54:55], v[22:23], v[176:177], v[54:55]\n"
"v_pk_fma_f32 v[48:49], v[24:25], v[210:211], v[48:49]\n"
"v_pk_fma_f32 v[50:51], v[26:27], v[218:219], v[50:51]\n"
"v_pk_fma_f32 v[52:53], v[28:29], v[226:227], v[52:53]\n"
"v_pk_fma_f32 v[54:55], v[30:31], v[234:235], v[54:55]\n"
"v_pk_fma_f32 v[48:49], v[32:33], v[208:209], v[48:49]\n"
"v_pk_fma_f32 v[50:51], v[34:35], v[216:217], v[50:51]\n"
"v_pk_fma_f32 v[52:53], v[36:37], v[224:225], v[52:53]\n"
"v_pk_fma_f32 v[54:55], v[38:39], v[232:233], v[54:55]\n"
"s_waitcnt vmcnt(11)\n"
"v_lshlrev_b32 v40, 16, v80\n"
"v_and_b32 v41, 0xffff0000, v80\n"
"v_lshlrev_b32 v42, 16, v81\n"
"v_and_b32 v43, 0xffff0000, v81\n"
"v_lshlrev_b32 v44, 16, v82\n"
"v_and_b32 v45, 0xffff0000, v82\n"
"v_lshlrev_b32 v46, 16, v83\n"
"v_and_b32 v47, 0xffff0000, v83\n"
"buffer_load_dwordx4 v[124:127], v5, s[48:51], s66 offen offset:2048\n"
"v_pk_mul_f32 v[48:49], v[48:49], v[40:41]\n"
"v_pk_mul_f32 v[50:51], v[50:51], v[42:43]\n"
"v_pk_mul_f32 v[52:53], v[52:53], v[44:45]\n"
"v_pk_mul_f32 v[54:55], v[54:55], v[46:47]\n"
"s_waitcnt vmcnt(11)\n"
"v_lshlrev_b32 v8, 16, v84\n"
"v_and_b32 v9, 0xffff0000, v84\n"
"v_lshlrev_b32 v10, 16, v85\n"
"v_and_b32 v11, 0xffff0000, v85\n"
"v_lshlrev_b32 v12, 16, v86\n"
"v_and_b32 v13, 0xffff0000, v86\n"
"v_lshlrev_b32 v14, 16, v87\n"
"v_and_b32 v15, 0xffff0000, v87\n"
"buffer_load_dwordx4 v[64:67], v5, s[52:55], s66 offen offset:2048\n"
"v_pk_mul_f32 v[56:57], v[16:17], v[214:215]\n"
"v_pk_mul_f32 v[58:59], v[18:19], v[222:223]\n"
"v_pk_mul_f32 v[60:61], v[20:21], v[230:231]\n"
"v_pk_mul_f32 v[62:63], v[22:23], v[178:179]\n"
"v_pk_fma_f32 v[56:57], v[24:25], v[212:213], v[56:57]\n"
"v_pk_fma_f32 v[58:59], v[26:27], v[220:221], v[58:59]\n"
"v_pk_fma_f32 v[60:61], v[28:29], v[228:229], v[60:61]\n"
"v_pk_fma_f32 v[62:63], v[30:31], v[176:177], v[62:63]\n"
"v_pk_fma_f32 v[56:57], v[32:33], v[210:211], v[56:57]\n"
"v_pk_fma_f32 v[58:59], v[34:35], v[218:219], v[58:59]\n"
"v_pk_fma_f32 v[60:61], v[36:37], v[226:227], v[60:61]\n"
"v_pk_fma_f32 v[62:63], v[38:39], v[234:235], v[62:63]\n"
"v_pk_fma_f32 v[56:57], v[8:9], v[208:209], v[56:57]\n"
"v_pk_fma_f32 v[58:59], v[10:11], v[216:217], v[58:59]\n"
"v_pk_fma_f32 v[60:61], v[12:13], v[224:225], v[60:61]\n"
"v_pk_fma_f32 v[62:63], v[14:15], v[232:233], v[62:63]\n"
"s_waitcnt vmcnt(11)\n"
"v_lshlrev_b32 v40, 16, v88\n"
"v_and_b32 v41, 0xffff0000, v88\n"
"v_lshlrev_b32 v42, 16, v89\n"
"v_and_b32 v43, 0xffff0000, v89\n"
"v_lshlrev_b32 v44, 16, v90\n"
"v_and_b32 v45, 0xffff0000, v90\n"
"v_lshlrev_b32 v46, 16, v91\n"
"v_and_b32 v47, 0xffff0000, v91\n"
"buffer_load_dwordx4 v[68:71], v5, s[48:51], s66 offen offset:3072\n"
"v_pk_mul_f32 v[56:57], v[56:57], v[40:41]\n"
"v_pk_mul_f32 v[58:59], v[58:59], v[42:43]\n"
"v_pk_mul_f32 v[60:61], v[60:61], v[44:45]\n"
"v_pk_mul_f32 v[62:63], v[62:63], v[46:47]\n"
"v_cvt_pk_bf16_f32 v131, v56, v48\n"
"v_cvt_pk_bf16_f32 v135, v57, v49\n"
"v_cvt_pk_bf16_f32 v139, v58, v50\n"
"v_cvt_pk_bf16_f32 v143, v59, v51\n"
"v_cvt_pk_bf16_f32 v147, v60, v52\n"
"v_cvt_pk_bf16_f32 v151, v61, v53\n"
"v_cvt_pk_bf16_f32 v155, v62, v54\n"
"v_cvt_pk_bf16_f32 v159, v63, v55\n"
"s_waitcnt vmcnt(11)\n"
"v_lshlrev_b32 v16, 16, v92\n"
"v_and_b32 v17, 0xffff0000, v92\n"
"v_lshlrev_b32 v18, 16, v93\n"
"v_and_b32 v19, 0xffff0000, v93\n"
"v_lshlrev_b32 v20, 16, v94\n"
"v_and_b32 v21, 0xffff0000, v94\n"
"v_lshlrev_b32 v22, 16, v95\n"
"v_and_b32 v23, 0xffff0000, v95\n"
"buffer_load_dwordx4 v[72:75], v5, s[52:55], s66 offen offset:3072\n"
"v_pk_mul_f32 v[48:49], v[24:25], v[214:215]\n"
"v_pk_mul_f32 v[50:51], v[26:27], v[222:223]\n"
"v_pk_mul_f32 v[52:53], v[28:29], v[230:231]\n"
"v_pk_mul_f32 v[54:55], v[30:31], v[178:179]\n"
"v_pk_fma_f32 v[48:49], v[32:33], v[212:213], v[48:49]\n"
"v_pk_fma_f32 v[50:51], v[34:35], v[220:221], v[50:51]\n"
"v_pk_fma_f32 v[52:53], v[36:37], v[228:229], v[52:53]\n"
"v_pk_fma_f32 v[54:55], v[38:39], v[176:177], v[54:55]\n"
"v_pk_fma_f32 v[48:49], v[8:9], v[210:211], v[48:49]\n"
"v_pk_fma_f32 v[50:51], v[10:11], v[218:219], v[50:51]\n"
"v_pk_fma_f32 v[52:53], v[12:13], v[226:227], v[52:53]\n"
"v_pk_fma_f32 v[54:55], v[14:15], v[234:235], v[54:55]\n"
"v_pk_fma_f32 v[48:49], v[16:17], v[208:209], v[48:49]\n"
"v_pk_fma_f32 v[50:51], v[18:19], v[216:217], v[50:51]\n"
"v_pk_fma_f32 v[52:53], v[20:21], v[224:225], v[52:53]\n"
"v_pk_fma_f32 v[54:55], v[22:23], v[232:233], v[54:55]\n"
"s_waitcnt vmcnt(11)\n"
"v_lshlrev_b32 v40, 16, v96\n"
"v_and_b32 v41, 0xffff0000, v96\n"
"v_lshlrev_b32 v42, 16, v97\n"
"v_and_b32 v43, 0xffff0000, v97\n"
"v_lshlrev_b32 v44, 16, v98\n"
"v_and_b32 v45, 0xffff0000, v98\n"
"v_lshlrev_b32 v46, 16, v99\n"
"v_and_b32 v47, 0xffff0000, v99\n"
"buffer_load_dwordx4 v[76:79], v5, s[48:51], s67 offen offset:0\n"
"v_pk_mul_f32 v[48:49], v[48:49], v[40:41]\n"
"v_pk_mul_f32 v[50:51], v[50:51], v[42:43]\n"
"v_pk_mul_f32 v[52:53], v[52:53], v[44:45]\n"
"v_pk_mul_f32 v[54:55], v[54:55], v[46:47]\n"
"s_waitcnt vmcnt(10)\n"
"v_lshlrev_b32 v24, 16, v100\n"
"v_and_b32 v25, 0xffff0000, v100\n"
"v_lshlrev_b32 v26, 16, v101\n"
"v_and_b32 v27, 0xffff0000, v101\n"
"v_lshlrev_b32 v28, 16, v102\n"
"v_and_b32 v29, 0xffff0000, v102\n"
"v_lshlrev_b32 v30, 16, v103\n"
"v_and_b32 v31, 0xffff0000, v103\n"
"buffer_load_dwordx4 v[80:83], v5, s[52:55], s67 offen offset:0\n"
"v_pk_mul_f32 v[56:57], v[32:33], v[214:215]\n"
"v_pk_mul_f32 v[58:59], v[34:35], v[222:223]\n"
"v_pk_mul_f32 v[60:61], v[36:37], v[230:231]\n"
"v_pk_mul_f32 v[62:63], v[38:39], v[178:179]\n"
"v_pk_fma_f32 v[56:57], v[8:9], v[212:213], v[56:57]\n"
"v_pk_fma_f32 v[58:59], v[10:11], v[220:221], v[58:59]\n"
"v_pk_fma_f32 v[60:61], v[12:13], v[228:229], v[60:61]\n"
"v_pk_fma_f32 v[62:63], v[14:15], v[176:177], v[62:63]\n"
"v_pk_fma_f32 v[56:57], v[16:17], v[210:211], v[56:57]\n"
"v_pk_fma_f32 v[58:59], v[18:19], v[218:219], v[58:59]\n"
"v_pk_fma_f32 v[60:61], v[20:21], v[226:227], v[60:61]\n"
"v_pk_fma_f32 v[62:63], v[22:23], v[234:235], v[62:63]\n"
"v_pk_fma_f32 v[56:57], v[24:25], v[208:209], v[56:57]\n"
"v_pk_fma_f32 v[58:59], v[26:27], v[216:217], v[58:59]\n"
"v_pk_fma_f32 v[60:61], v[28:29], v[224:225], v[60:61]\n"
"v_pk_fma_f32 v[62:63], v[30:31], v[232:233], v[62:63]\n"
"s_waitcnt vmcnt(10)\n"
"v_lshlrev_b32 v40, 16, v104\n"
"v_and_b32 v41, 0xffff0000, v104\n"
"v_lshlrev_b32 v42, 16, v105\n"
"v_and_b32 v43, 0xffff0000, v105\n"
"v_lshlrev_b32 v44, 16, v106\n"
"v_and_b32 v45, 0xffff0000, v106\n"
"v_lshlrev_b32 v46, 16, v107\n"
"v_and_b32 v47, 0xffff0000, v107\n"
"buffer_load_dwordx4 v[84:87], v5, s[48:51], s67 offen offset:1024\n"
"v_pk_mul_f32 v[56:57], v[56:57], v[40:41]\n"
"v_pk_mul_f32 v[58:59], v[58:59], v[42:43]\n"
"v_pk_mul_f32 v[60:61], v[60:61], v[44:45]\n"
"v_pk_mul_f32 v[62:63], v[62:63], v[46:47]\n"
"v_cvt_pk_bf16_f32 v130, v56, v48\n"
"v_cvt_pk_bf16_f32 v134, v57, v49\n"
"v_cvt_pk_bf16_f32 v138, v58, v50\n"
"v_cvt_pk_bf16_f32 v142, v59, v51\n"
"v_cvt_pk_bf16_f32 v146, v60, v52\n"
"v_cvt_pk_bf16_f32 v150, v61, v53\n"
"v_cvt_pk_bf16_f32 v154, v62, v54\n"
"v_cvt_pk_bf16_f32 v158, v63, v55\n"
"s_waitcnt vmcnt(10)\n"
"v_lshlrev_b32 v32, 16, v108\n"
"v_and_b32 v33, 0xffff0000, v108\n"
"v_lshlrev_b32 v34, 16, v109\n"
"v_and_b32 v35, 0xffff0000, v109\n"
"v_lshlrev_b32 v36, 16, v110\n"
"v_and_b32 v37, 0xffff0000, v110\n"
"v_lshlrev_b32 v38, 16, v111\n"
"v_and_b32 v39, 0xffff0000, v111\n"
"buffer_load_dwordx4 v[88:91], v5, s[52:55], s67 offen offset:1024\n"
"v_pk_mul_f32 v[48:49], v[8:9], v[214:215]\n"
"v_pk_mul_f32 v[50:51], v[10:11], v[222:223]\n"
"v_pk_mul_f32 v[52:53], v[12:13], v[230:231]\n"
"v_pk_mul_f32 v[54:55], v[14:15], v[178:179]\n"
"v_pk_fma_f32 v[48:49], v[16:17], v[212:213], v[48:49]\n"
"v_pk_fma_f32 v[50:51], v[18:19], v[220:221], v[50:51]\n"
"v_pk_fma_f32 v[52:53], v[20:21], v[228:229], v[52:53]\n"
"v_pk_fma_f32 v[54:55], v[22:23], v[176:177], v[54:55]\n"
"v_pk_fma_f32 v[48:49], v[24:25], v[210:211], v[48:49]\n"
"v_pk_fma_f32 v[50:51], v[26:27], v[218:219], v[50:51]\n"
"v_pk_fma_f32 v[52:53], v[28:29], v[226:227], v[52:53]\n"
"v_pk_fma_f32 v[54:55], v[30:31], v[234:235], v[54:55]\n"
"v_pk_fma_f32 v[48:49], v[32:33], v[208:209], v[48:49]\n"
"v_pk_fma_f32 v[50:51], v[34:35], v[216:217], v[50:51]\n"
"v_pk_fma_f32 v[52:53], v[36:37], v[224:225], v[52:53]\n"
"v_pk_fma_f32 v[54:55], v[38:39], v[232:233], v[54:55]\n"
"s_waitcnt vmcnt(10)\n"
"v_lshlrev_b32 v40, 16, v112\n"
"v_and_b32 v41, 0xffff0000, v112\n"
"v_lshlrev_b32 v42, 16, v113\n"
"v_and_b32 v43, 0xffff0000, v113\n"
"v_lshlrev_b32 v44, 16, v114\n"
"v_and_b32 v45, 0xffff0000, v114\n"
"v_lshlrev_b32 v46, 16, v115\n"
"v_and_b32 v47, 0xffff0000, v115\n"
"buffer_load_dwordx4 v[92:95], v5, s[48:51], s67 offen offset:2048\n"
"v_pk_mul_f32 v[48:49], v[48:49], v[40:41]\n"
"v_pk_mul_f32 v[50:51], v[50:51], v[42:43]\n"
"v_pk_mul_f32 v[52:53], v[52:53], v[44:45]\n"
"v_pk_mul_f32 v[54:55], v[54:55], v[46:47]\n"
"s_waitcnt vmcnt(10)\n"
"v_lshlrev_b32 v8, 16, v116\n"
"v_and_b32 v9, 0xffff0000, v116\n"
"v_lshlrev_b32 v10, 16, v117\n"
"v_and_b32 v11, 0xffff0000, v117\n"
"v_lshlrev_b32 v12, 16, v118\n"
"v_and_b32 v13, 0xffff0000, v118\n"
"v_lshlrev_b32 v14, 16, v119\n"
"v_and_b32 v15, 0xffff0000, v119\n"
"buffer_load_dwordx4 v[96:99], v5, s[52:55], s67 offen offset:2048\n"
"v_pk_mul_f32 v[56:57], v[16:17], v[214:215]\n"
"v_pk_mul_f32 v[58:59], v[18:19], v[222:223]\n"
"v_pk_mul_f32 v[60:61], v[20:21], v[230:231]\n"
"v_pk_mul_f32 v[62:63], v[22:23], v[178:179]\n"
"v_pk_fma_f32 v[56:57], v[24:25], v[212:213], v[56:57]\n"
"v_pk_fma_f32 v[58:59], v[26:27], v[220:221], v[58:59]\n"
"v_pk_fma_f32 v[60:61], v[28:29], v[228:229], v[60:61]\n"
"v_pk_fma_f32 v[62:63], v[30:31], v[176:177], v[62:63]\n"
"v_pk_fma_f32 v[56:57], v[32:33], v[210:211], v[56:57]\n"
"v_pk_fma_f32 v[58:59], v[34:35], v[218:219], v[58:59]\n"
"v_pk_fma_f32 v[60:61], v[36:37], v[226:227], v[60:61]\n"
"v_pk_fma_f32 v[62:63], v[38:39], v[234:235], v[62:63]\n"
"v_pk_fma_f32 v[56:57], v[8:9], v[208:209], v[56:57]\n"
"v_pk_fma_f32 v[58:59], v[10:11], v[216:217], v[58:59]\n"
"v_pk_fma_f32 v[60:61], v[12:13], v[224:225], v[60:61]\n"
"v_pk_fma_f32 v[62:63], v[14:15], v[232:233], v[62:63]\n"
"s_waitcnt vmcnt(10)\n"
"v_lshlrev_b32 v40, 16, v120\n"
"v_and_b32 v41, 0xffff0000, v120\n"
"v_lshlrev_b32 v42, 16, v121\n"
"v_and_b32 v43, 0xffff0000, v121\n"
"v_lshlrev_b32 v44, 16, v122\n"
"v_and_b32 v45, 0xffff0000, v122\n"
"v_lshlrev_b32 v46, 16, v123\n"
"v_and_b32 v47, 0xffff0000, v123\n"
"buffer_load_dwordx4 v[100:103], v5, s[48:51], s67 offen offset:3072\n"
"v_pk_mul_f32 v[56:57], v[56:57], v[40:41]\n"
"v_pk_mul_f32 v[58:59], v[58:59], v[42:43]\n"
"v_pk_mul_f32 v[60:61], v[60:61], v[44:45]\n"
"v_pk_mul_f32 v[62:63], v[62:63], v[46:47]\n"
"v_cvt_pk_bf16_f32 v129, v56, v48\n"
"v_cvt_pk_bf16_f32 v133, v57, v49\n"
"v_cvt_pk_bf16_f32 v137, v58, v50\n"
"v_cvt_pk_bf16_f32 v141, v59, v51\n"
"v_cvt_pk_bf16_f32 v145, v60, v52\n"
"v_cvt_pk_bf16_f32 v149, v61, v53\n"
"v_cvt_pk_bf16_f32 v153, v62, v54\n"
"v_cvt_pk_bf16_f32 v157, v63, v55\n"
"s_waitcnt vmcnt(10)\n"
"v_lshlrev_b32 v16, 16, v124\n"
"v_and_b32 v17, 0xffff0000, v124\n"
"v_lshlrev_b32 v18, 16, v125\n"
"v_and_b32 v19, 0xffff0000, v125\n"
"v_lshlrev_b32 v20, 16, v126\n"
"v_and_b32 v21, 0xffff0000, v126\n"
"v_lshlrev_b32 v22, 16, v127\n"
"v_and_b32 v23, 0xffff0000, v127\n"
"buffer_load_dwordx4 v[104:107], v5, s[52:55], s67 offen offset:3072\n"
"v_pk_mul_f32 v[48:49], v[24:25], v[214:215]\n"
"v_pk_mul_f32 v[50:51], v[26:27], v[222:223]\n"
"v_pk_mul_f32 v[52:53], v[28:29], v[230:231]\n"
"v_pk_mul_f32 v[54:55], v[30:31], v[178:179]\n"
"v_pk_fma_f32 v[48:49], v[32:33], v[212:213], v[48:49]\n"
"v_pk_fma_f32 v[50:51], v[34:35], v[220:221], v[50:51]\n"
"v_pk_fma_f32 v[52:53], v[36:37], v[228:229], v[52:53]\n"
"v_pk_fma_f32 v[54:55], v[38:39], v[176:177], v[54:55]\n"
"v_pk_fma_f32 v[48:49], v[8:9], v[210:211], v[48:49]\n"
"v_pk_fma_f32 v[50:51], v[10:11], v[218:219], v[50:51]\n"
"v_pk_fma_f32 v[52:53], v[12:13], v[226:227], v[52:53]\n"
"v_pk_fma_f32 v[54:55], v[14:15], v[234:235], v[54:55]\n"
"v_pk_fma_f32 v[48:49], v[16:17], v[208:209], v[48:49]\n"
"v_pk_fma_f32 v[50:51], v[18:19], v[216:217], v[50:51]\n"
"v_pk_fma_f32 v[52:53], v[20:21], v[224:225], v[52:53]\n"
"v_pk_fma_f32 v[54:55], v[22:23], v[232:233], v[54:55]\n"
"s_waitcnt vmcnt(10)\n"
"v_lshlrev_b32 v40, 16, v64\n"
"v_and_b32 v41, 0xffff0000, v64\n"
"v_lshlrev_b32 v42, 16, v65\n"
"v_and_b32 v43, 0xffff0000, v65\n"
"v_lshlrev_b32 v44, 16, v66\n"
"v_and_b32 v45, 0xffff0000, v66\n"
"v_lshlrev_b32 v46, 16, v67\n"
"v_and_b32 v47, 0xffff0000, v67\n"
"buffer_load_dwordx4 v[108:111], v5, s[48:51], s74 offen offset:0\n"
"v_pk_mul_f32 v[48:49], v[48:49], v[40:41]\n"
"v_pk_mul_f32 v[50:51], v[50:51], v[42:43]\n"
"v_pk_mul_f32 v[52:53], v[52:53], v[44:45]\n"
"v_pk_mul_f32 v[54:55], v[54:55], v[46:47]\n"
"s_waitcnt vmcnt(10)\n"
"v_lshlrev_b32 v24, 16, v68\n"
"v_and_b32 v25, 0xffff0000, v68\n"
"v_lshlrev_b32 v26, 16, v69\n"
"v_and_b32 v27, 0xffff0000, v69\n"
"v_lshlrev_b32 v28, 16, v70\n"
"v_and_b32 v29, 0xffff0000, v70\n"
"v_lshlrev_b32 v30, 16, v71\n"
"v_and_b32 v31, 0xffff0000, v71\n"
"buffer_load_dwordx4 v[112:115], v5, s[52:55], s74 offen offset:0\n"
"v_pk_mul_f32 v[56:57], v[32:33], v[214:215]\n"
"v_pk_mul_f32 v[58:59], v[34:35], v[222:223]\n"
"v_pk_mul_f32 v[60:61], v[36:37], v[230:231]\n"
"v_pk_mul_f32 v[62:63], v[38:39], v[178:179]\n"
"v_pk_fma_f32 v[56:57], v[8:9], v[212:213], v[56:57]\n"
"v_pk_fma_f32 v[58:59], v[10:11], v[220:221], v[58:59]\n"
"v_pk_fma_f32 v[60:61], v[12:13], v[228:229], v[60:61]\n"
"v_pk_fma_f32 v[62:63], v[14:15], v[176:177], v[62:63]\n"
"v_pk_fma_f32 v[56:57], v[16:17], v[210:211], v[56:57]\n"
"v_pk_fma_f32 v[58:59], v[18:19], v[218:219], v[58:59]\n"
"v_pk_fma_f32 v[60:61], v[20:21], v[226:227], v[60:61]\n"
"v_pk_fma_f32 v[62:63], v[22:23], v[234:235], v[62:63]\n"
"v_pk_fma_f32 v[56:57], v[24:25], v[208:209], v[56:57]\n"
"v_pk_fma_f32 v[58:59], v[26:27], v[216:217], v[58:59]\n"
"v_pk_fma_f32 v[60:61], v[28:29], v[224:225], v[60:61]\n"
"v_pk_fma_f32 v[62:63], v[30:31], v[232:233], v[62:63]\n"
"s_waitcnt vmcnt(10)\n"
"v_lshlrev_b32 v40, 16, v72\n"
"v_and_b32 v41, 0xffff0000, v72\n"
"v_lshlrev_b32 v42, 16, v73\n"
"v_and_b32 v43, 0xffff0000, v73\n"
"v_lshlrev_b32 v44, 16, v74\n"
"v_and_b32 v45, 0xffff0000, v74\n"
"v_lshlrev_b32 v46, 16, v75\n"
"v_and_b32 v47, 0xffff0000, v75\n"
"buffer_load_dwordx4 v[116:119], v5, s[48:51], s74 offen offset:1024\n"
"v_pk_mul_f32 v[56:57], v[56:57], v[40:41]\n"
"v_pk_mul_f32 v[58:59], v[58:59], v[42:43]\n"
"v_pk_mul_f32 v[60:61], v[60:61], v[44:45]\n"
"v_pk_mul_f32 v[62:63], v[62:63], v[46:47]\n"
"v_cvt_pk_bf16_f32 v128, v56, v48\n"
"v_cvt_pk_bf16_f32 v132, v57, v49\n"
"v_cvt_pk_bf16_f32 v136, v58, v50\n"
"v_cvt_pk_bf16_f32 v140, v59, v51\n"
"v_cvt_pk_bf16_f32 v144, v60, v52\n"
"v_cvt_pk_bf16_f32 v148, v61, v53\n"
"v_cvt_pk_bf16_f32 v152, v62, v54\n"
"v_cvt_pk_bf16_f32 v156, v63, v55\n"
"ds_write_b128 v206, v[128:131] offset:0\n"
"ds_write_b128 v206, v[132:135] offset:128\n"
"ds_write_b128 v206, v[136:139] offset:256\n"
"ds_write_b128 v206, v[140:143] offset:384\n"
"ds_write_b128 v206, v[144:147] offset:512\n"
"ds_write_b128 v206, v[148:151] offset:640\n"
"ds_write_b128 v206, v[152:155] offset:768\n"
"ds_write_b128 v206, v[156:159] offset:896\n"
"s_waitcnt lgkmcnt(0)\n"
"ds_read_b128 v[128:131], v207 offset:0\n"
"ds_read_b128 v[132:135], v207 offset:128\n"
"ds_read_b128 v[136:139], v207 offset:256\n"
"ds_read_b128 v[140:143], v207 offset:384\n"
"ds_read_b128 v[144:147], v207 offset:512\n"
"ds_read_b128 v[148:151], v207 offset:640\n"
"ds_read_b128 v[152:155], v207 offset:768\n"
"ds_read_b128 v[156:159], v207 offset:896\n"
"s_waitcnt vmcnt(10)\n"
"v_lshlrev_b32 v32, 16, v76\n"
"v_and_b32 v33, 0xffff0000, v76\n"
"v_lshlrev_b32 v34, 16, v77\n"
"v_and_b32 v35, 0xffff0000, v77\n"
"v_lshlrev_b32 v36, 16, v78\n"
"v_and_b32 v37, 0xffff0000, v78\n"
"v_lshlrev_b32 v38, 16, v79\n"
"v_and_b32 v39, 0xffff0000, v79\n"
"buffer_load_dwordx4 v[120:123], v5, s[52:55], s74 offen offset:1024\n"
"v_pk_mul_f32 v[48:49], v[8:9], v[214:215]\n"
"v_pk_mul_f32 v[50:51], v[10:11], v[222:223]\n"
"v_pk_mul_f32 v[52:53], v[12:13], v[230:231]\n"
"v_pk_mul_f32 v[54:55], v[14:15], v[178:179]\n"
"v_pk_fma_f32 v[48:49], v[16:17], v[212:213], v[48:49]\n"
"v_pk_fma_f32 v[50:51], v[18:19], v[220:221], v[50:51]\n"
"v_pk_fma_f32 v[52:53], v[20:21], v[228:229], v[52:53]\n"
"v_pk_fma_f32 v[54:55], v[22:23], v[176:177], v[54:55]\n"
"v_pk_fma_f32 v[48:49], v[24:25], v[210:211], v[48:49]\n"
"v_pk_fma_f32 v[50:51], v[26:27], v[218:219], v[50:51]\n"
"v_pk_fma_f32 v[52:53], v[28:29], v[226:227], v[52:53]\n"
"v_pk_fma_f32 v[54:55], v[30:31], v[234:235], v[54:55]\n"
"v_pk_fma_f32 v[48:49], v[32:33], v[208:209], v[48:49]\n"
"v_pk_fma_f32 v[50:51], v[34:35], v[216:217], v[50:51]\n"
"v_pk_fma_f32 v[52:53], v[36:37], v[224:225], v[52:53]\n"
"v_pk_fma_f32 v[54:55], v[38:39], v[232:233], v[54:55]\n"
"s_waitcnt vmcnt(10)\n"
"v_lshlrev_b32 v40, 16, v80\n"
"v_and_b32 v41, 0xffff0000, v80\n"
"v_lshlrev_b32 v42, 16, v81\n"
"v_and_b32 v43, 0xffff0000, v81\n"
"v_lshlrev_b32 v44, 16, v82\n"
"v_and_b32 v45, 0xffff0000, v82\n"
"v_lshlrev_b32 v46, 16, v83\n"
"v_and_b32 v47, 0xffff0000, v83\n"
"buffer_load_dwordx4 v[124:127], v5, s[48:51], s74 offen offset:2048\n"
"v_pk_mul_f32 v[48:49], v[48:49], v[40:41]\n"
"v_pk_mul_f32 v[50:51], v[50:51], v[42:43]\n"
"v_pk_mul_f32 v[52:53], v[52:53], v[44:45]\n"
"v_pk_mul_f32 v[54:55], v[54:55], v[46:47]\n"
"s_waitcnt vmcnt(10)\n"
"v_lshlrev_b32 v8, 16, v84\n"
"v_and_b32 v9, 0xffff0000, v84\n"
"v_lshlrev_b32 v10, 16, v85\n"
"v_and_b32 v11, 0xffff0000, v85\n"
"v_lshlrev_b32 v12, 16, v86\n"
"v_and_b32 v13, 0xffff0000, v86\n"
"v_lshlrev_b32 v14, 16, v87\n"
"v_and_b32 v15, 0xffff0000, v87\n"
"buffer_load_dwordx4 v[64:67], v5, s[52:55], s74 offen offset:2048\n"
"v_pk_mul_f32 v[56:57], v[16:17], v[214:215]\n"
"v_pk_mul_f32 v[58:59], v[18:19], v[222:223]\n"
"v_pk_mul_f32 v[60:61], v[20:21], v[230:231]\n"
"v_pk_mul_f32 v[62:63], v[22:23], v[178:179]\n"
"v_pk_fma_f32 v[56:57], v[24:25], v[212:213], v[56:57]\n"
"v_pk_fma_f32 v[58:59], v[26:27], v[220:221], v[58:59]\n"
"v_pk_fma_f32 v[60:61], v[28:29], v[228:229], v[60:61]\n"
"v_pk_fma_f32 v[62:63], v[30:31], v[176:177], v[62:63]\n"
"v_pk_fma_f32 v[56:57], v[32:33], v[210:211], v[56:57]\n"
"v_pk_fma_f32 v[58:59], v[34:35], v[218:219], v[58:59]\n"
"v_pk_fma_f32 v[60:61], v[36:37], v[226:227], v[60:61]\n"
"v_pk_fma_f32 v[62:63], v[38:39], v[234:235], v[62:63]\n"
"v_pk_fma_f32 v[56:57], v[8:9], v[208:209], v[56:57]\n"
"v_pk_fma_f32 v[58:59], v[10:11], v[216:217], v[58:59]\n"
"v_pk_fma_f32 v[60:61], v[12:13], v[224:225], v[60:61]\n"
"v_pk_fma_f32 v[62:63], v[14:15], v[232:233], v[62:63]\n"
"s_waitcnt vmcnt(10)\n"
"v_lshlrev_b32 v40, 16, v88\n"
"v_and_b32 v41, 0xffff0000, v88\n"
"v_lshlrev_b32 v42, 16, v89\n"
"v_and_b32 v43, 0xffff0000, v89\n"
"v_lshlrev_b32 v44, 16, v90\n"
"v_and_b32 v45, 0xffff0000, v90\n"
"v_lshlrev_b32 v46, 16, v91\n"
"v_and_b32 v47, 0xffff0000, v91\n"
"buffer_load_dwordx4 v[68:71], v5, s[48:51], s74 offen offset:3072\n"
"v_pk_mul_f32 v[56:57], v[56:57], v[40:41]\n"
"v_pk_mul_f32 v[58:59], v[58:59], v[42:43]\n"
"v_pk_mul_f32 v[60:61], v[60:61], v[44:45]\n"
"v_pk_mul_f32 v[62:63], v[62:63], v[46:47]\n"
"v_cvt_pk_bf16_f32 v163, v56, v48\n"
"v_cvt_pk_bf16_f32 v167, v57, v49\n"
"v_cvt_pk_bf16_f32 v171, v58, v50\n"
"v_cvt_pk_bf16_f32 v175, v59, v51\n"
"v_cvt_pk_bf16_f32 v183, v60, v52\n"
"v_cvt_pk_bf16_f32 v187, v61, v53\n"
"v_cvt_pk_bf16_f32 v191, v62, v54\n"
"v_cvt_pk_bf16_f32 v195, v63, v55\n"
"s_waitcnt vmcnt(10)\n"
"v_lshlrev_b32 v16, 16, v92\n"
"v_and_b32 v17, 0xffff0000, v92\n"
"v_lshlrev_b32 v18, 16, v93\n"
"v_and_b32 v19, 0xffff0000, v93\n"
"v_lshlrev_b32 v20, 16, v94\n"
"v_and_b32 v21, 0xffff0000, v94\n"
"v_lshlrev_b32 v22, 16, v95\n"
"v_and_b32 v23, 0xffff0000, v95\n"
"buffer_load_dwordx4 v[72:75], v5, s[52:55], s74 offen offset:3072\n"
"v_pk_mul_f32 v[48:49], v[24:25], v[214:215]\n"
"v_pk_mul_f32 v[50:51], v[26:27], v[222:223]\n"
"v_pk_mul_f32 v[52:53], v[28:29], v[230:231]\n"
"v_pk_mul_f32 v[54:55], v[30:31], v[178:179]\n"
"v_pk_fma_f32 v[48:49], v[32:33], v[212:213], v[48:49]\n"
"v_pk_fma_f32 v[50:51], v[34:35], v[220:221], v[50:51]\n"
"v_pk_fma_f32 v[52:53], v[36:37], v[228:229], v[52:53]\n"
"v_pk_fma_f32 v[54:55], v[38:39], v[176:177], v[54:55]\n"
"v_pk_fma_f32 v[48:49], v[8:9], v[210:211], v[48:49]\n"
"v_pk_fma_f32 v[50:51], v[10:11], v[218:219], v[50:51]\n"
"v_pk_fma_f32 v[52:53], v[12:13], v[226:227], v[52:53]\n"
"v_pk_fma_f32 v[54:55], v[14:15], v[234:235], v[54:55]\n"
"v_pk_fma_f32 v[48:49], v[16:17], v[208:209], v[48:49]\n"
"v_pk_fma_f32 v[50:51], v[18:19], v[216:217], v[50:51]\n"
"v_pk_fma_f32 v[52:53], v[20:21], v[224:225], v[52:53]\n"
"v_pk_fma_f32 v[54:55], v[22:23], v[232:233], v[54:55]\n"
"s_waitcnt vmcnt(10)\n"
"v_lshlrev_b32 v40, 16, v96\n"
"v_and_b32 v41, 0xffff0000, v96\n"
"v_lshlrev_b32 v42, 16, v97\n"
"v_and_b32 v43, 0xffff0000, v97\n"
"v_lshlrev_b32 v44, 16, v98\n"
"v_and_b32 v45, 0xffff0000, v98\n"
"v_lshlrev_b32 v46, 16, v99\n"
"v_and_b32 v47, 0xffff0000, v99\n"
"v_pk_mul_f32 v[48:49], v[48:49], v[40:41]\n"
"v_pk_mul_f32 v[50:51], v[50:51], v[42:43]\n"
"v_pk_mul_f32 v[52:53], v[52:53], v[44:45]\n"
"v_pk_mul_f32 v[54:55], v[54:55], v[46:47]\n"
"s_waitcnt vmcnt(9)\n"
"v_lshlrev_b32 v24, 16, v100\n"
"v_and_b32 v25, 0xffff0000, v100\n"
"v_lshlrev_b32 v26, 16, v101\n"
"v_and_b32 v27, 0xffff0000, v101\n"
"v_lshlrev_b32 v28, 16, v102\n"
"v_and_b32 v29, 0xffff0000, v102\n"
"v_lshlrev_b32 v30, 16, v103\n"
"v_and_b32 v31, 0xffff0000, v103\n"
"v_pk_mul_f32 v[56:57], v[32:33], v[214:215]\n"
"v_pk_mul_f32 v[58:59], v[34:35], v[222:223]\n"
"v_pk_mul_f32 v[60:61], v[36:37], v[230:231]\n"
"v_pk_mul_f32 v[62:63], v[38:39], v[178:179]\n"
"v_pk_fma_f32 v[56:57], v[8:9], v[212:213], v[56:57]\n"
"v_pk_fma_f32 v[58:59], v[10:11], v[220:221], v[58:59]\n"
"v_pk_fma_f32 v[60:61], v[12:13], v[228:229], v[60:61]\n"
"v_pk_fma_f32 v[62:63], v[14:15], v[176:177], v[62:63]\n"
"v_pk_fma_f32 v[56:57], v[16:17], v[210:211], v[56:57]\n"
"v_pk_fma_f32 v[58:59], v[18:19], v[218:219], v[58:59]\n"
"v_pk_fma_f32 v[60:61], v[20:21], v[226:227], v[60:61]\n"
"v_pk_fma_f32 v[62:63], v[22:23], v[234:235], v[62:63]\n"
"v_pk_fma_f32 v[56:57], v[24:25], v[208:209], v[56:57]\n"
"v_pk_fma_f32 v[58:59], v[26:27], v[216:217], v[58:59]\n"
"v_pk_fma_f32 v[60:61], v[28:29], v[224:225], v[60:61]\n"
"v_pk_fma_f32 v[62:63], v[30:31], v[232:233], v[62:63]\n"
"s_waitcnt vmcnt(8)\n"
"v_lshlrev_b32 v40, 16, v104\n"
"v_and_b32 v41, 0xffff0000, v104\n"
"v_lshlrev_b32 v42, 16, v105\n"
"v_and_b32 v43, 0xffff0000, v105\n"
"v_lshlrev_b32 v44, 16, v106\n"
"v_and_b32 v45, 0xffff0000, v106\n"
"v_lshlrev_b32 v46, 16, v107\n"
"v_and_b32 v47, 0xffff0000, v107\n"
"v_pk_mul_f32 v[56:57], v[56:57], v[40:41]\n"
"v_pk_mul_f32 v[58:59], v[58:59], v[42:43]\n"
"v_pk_mul_f32 v[60:61], v[60:61], v[44:45]\n"
"v_pk_mul_f32 v[62:63], v[62:63], v[46:47]\n"
"v_cvt_pk_bf16_f32 v162, v56, v48\n"
"v_cvt_pk_bf16_f32 v166, v57, v49\n"
"v_cvt_pk_bf16_f32 v170, v58, v50\n"
"v_cvt_pk_bf16_f32 v174, v59, v51\n"
"v_cvt_pk_bf16_f32 v182, v60, v52\n"
"v_cvt_pk_bf16_f32 v186, v61, v53\n"
"v_cvt_pk_bf16_f32 v190, v62, v54\n"
"v_cvt_pk_bf16_f32 v194, v63, v55\n"
"s_waitcnt vmcnt(7)\n"
"v_lshlrev_b32 v32, 16, v108\n"
"v_and_b32 v33, 0xffff0000, v108\n"
"v_lshlrev_b32 v34, 16, v109\n"
"v_and_b32 v35, 0xffff0000, v109\n"
"v_lshlrev_b32 v36, 16, v110\n"
"v_and_b32 v37, 0xffff0000, v110\n"
"v_lshlrev_b32 v38, 16, v111\n"
"v_and_b32 v39, 0xffff0000, v111\n"
"v_pk_mul_f32 v[48:49], v[8:9], v[214:215]\n"
"v_pk_mul_f32 v[50:51], v[10:11], v[222:223]\n"
"v_pk_mul_f32 v[52:53], v[12:13], v[230:231]\n"
"v_pk_mul_f32 v[54:55], v[14:15], v[178:179]\n"
"v_pk_fma_f32 v[48:49], v[16:17], v[212:213], v[48:49]\n"
"v_pk_fma_f32 v[50:51], v[18:19], v[220:221], v[50:51]\n"
"v_pk_fma_f32 v[52:53], v[20:21], v[228:229], v[52:53]\n"
"v_pk_fma_f32 v[54:55], v[22:23], v[176:177], v[54:55]\n"
"v_pk_fma_f32 v[48:49], v[24:25], v[210:211], v[48:49]\n"
"v_pk_fma_f32 v[50:51], v[26:27], v[218:219], v[50:51]\n"
"v_pk_fma_f32 v[52:53], v[28:29], v[226:227], v[52:53]\n"
"v_pk_fma_f32 v[54:55], v[30:31], v[234:235], v[54:55]\n"
"v_pk_fma_f32 v[48:49], v[32:33], v[208:209], v[48:49]\n"
"v_pk_fma_f32 v[50:51], v[34:35], v[216:217], v[50:51]\n"
"v_pk_fma_f32 v[52:53], v[36:37], v[224:225], v[52:53]\n"
"v_pk_fma_f32 v[54:55], v[38:39], v[232:233], v[54:55]\n"
"s_waitcnt vmcnt(6)\n"
"v_lshlrev_b32 v40, 16, v112\n"
"v_and_b32 v41, 0xffff0000, v112\n"
"v_lshlrev_b32 v42, 16, v113\n"
"v_and_b32 v43, 0xffff0000, v113\n"
"v_lshlrev_b32 v44, 16, v114\n"
"v_and_b32 v45, 0xffff0000, v114\n"
"v_lshlrev_b32 v46, 16, v115\n"
"v_and_b32 v47, 0xffff0000, v115\n"
"v_pk_mul_f32 v[48:49], v[48:49], v[40:41]\n"
"v_pk_mul_f32 v[50:51], v[50:51], v[42:43]\n"
"v_pk_mul_f32 v[52:53], v[52:53], v[44:45]\n"
"v_pk_mul_f32 v[54:55], v[54:55], v[46:47]\n"
"s_mov_b32 s63, 1048576\n"
"s_waitcnt lgkmcnt(0)\n"
"buffer_store_dwordx4 v[128:131], v252, s[20:23], s63 offen offset:0 sc1\n"
"s_mov_b32 s63, 1052672\n"
"buffer_store_dwordx4 v[132:135], v252, s[20:23], s63 offen offset:0 sc1\n"
"s_waitcnt vmcnt(7)\n"
"v_lshlrev_b32 v8, 16, v116\n"
"v_and_b32 v9, 0xffff0000, v116\n"
"v_lshlrev_b32 v10, 16, v117\n"
"v_and_b32 v11, 0xffff0000, v117\n"
"v_lshlrev_b32 v12, 16, v118\n"
"v_and_b32 v13, 0xffff0000, v118\n"
"v_lshlrev_b32 v14, 16, v119\n"
"v_and_b32 v15, 0xffff0000, v119\n"
"v_pk_mul_f32 v[56:57], v[16:17], v[214:215]\n"
"v_pk_mul_f32 v[58:59], v[18:19], v[222:223]\n"
"v_pk_mul_f32 v[60:61], v[20:21], v[230:231]\n"
"v_pk_mul_f32 v[62:63], v[22:23], v[178:179]\n"
"v_pk_fma_f32 v[56:57], v[24:25], v[212:213], v[56:57]\n"
"v_pk_fma_f32 v[58:59], v[26:27], v[220:221], v[58:59]\n"
"v_pk_fma_f32 v[60:61], v[28:29], v[228:229], v[60:61]\n"
"v_pk_fma_f32 v[62:63], v[30:31], v[176:177], v[62:63]\n"
"v_pk_fma_f32 v[56:57], v[32:33], v[210:211], v[56:57]\n"
"v_pk_fma_f32 v[58:59], v[34:35], v[218:219], v[58:59]\n"
"v_pk_fma_f32 v[60:61], v[36:37], v[226:227], v[60:61]\n"
"v_pk_fma_f32 v[62:63], v[38:39], v[234:235], v[62:63]\n"
"v_pk_fma_f32 v[56:57], v[8:9], v[208:209], v[56:57]\n"
"v_pk_fma_f32 v[58:59], v[10:11], v[216:217], v[58:59]\n"
"v_pk_fma_f32 v[60:61], v[12:13], v[224:225], v[60:61]\n"
"v_pk_fma_f32 v[62:63], v[14:15], v[232:233], v[62:63]\n"
"s_waitcnt vmcnt(6)\n"
"v_lshlrev_b32 v40, 16, v120\n"
"v_and_b32 v41, 0xffff0000, v120\n"
"v_lshlrev_b32 v42, 16, v121\n"
"v_and_b32 v43, 0xffff0000, v121\n"
"v_lshlrev_b32 v44, 16, v122\n"
"v_and_b32 v45, 0xffff0000, v122\n"
"v_lshlrev_b32 v46, 16, v123\n"
"v_and_b32 v47, 0xffff0000, v123\n"
"v_pk_mul_f32 v[56:57], v[56:57], v[40:41]\n"
"v_pk_mul_f32 v[58:59], v[58:59], v[42:43]\n"
"v_pk_mul_f32 v[60:61], v[60:61], v[44:45]\n"
"v_pk_mul_f32 v[62:63], v[62:63], v[46:47]\n"
"v_cvt_pk_bf16_f32 v161, v56, v48\n"
"v_cvt_pk_bf16_f32 v165, v57, v49\n"
"v_cvt_pk_bf16_f32 v169, v58, v50\n"
"v_cvt_pk_bf16_f32 v173, v59, v51\n"
"v_cvt_pk_bf16_f32 v181, v60, v52\n"
"v_cvt_pk_bf16_f32 v185, v61, v53\n"
"v_cvt_pk_bf16_f32 v189, v62, v54\n"
"v_cvt_pk_bf16_f32 v193, v63, v55\n"
"s_mov_b32 s63, 1056768\n"
"buffer_store_dwordx4 v[136:139], v252, s[20:23], s63 offen offset:0 sc1\n"
"s_mov_b32 s63, 1060864\n"
"buffer_store_dwordx4 v[140:143], v252, s[20:23], s63 offen offset:0 sc1\n"
"s_waitcnt vmcnt(7)\n"
"v_lshlrev_b32 v16, 16, v124\n"
"v_and_b32 v17, 0xffff0000, v124\n"
"v_lshlrev_b32 v18, 16, v125\n"
"v_and_b32 v19, 0xffff0000, v125\n"
"v_lshlrev_b32 v20, 16, v126\n"
"v_and_b32 v21, 0xffff0000, v126\n"
"v_lshlrev_b32 v22, 16, v127\n"
"v_and_b32 v23, 0xffff0000, v127\n"
"v_pk_mul_f32 v[48:49], v[24:25], v[214:215]\n"
"v_pk_mul_f32 v[50:51], v[26:27], v[222:223]\n"
"v_pk_mul_f32 v[52:53], v[28:29], v[230:231]\n"
"v_pk_mul_f32 v[54:55], v[30:31], v[178:179]\n"
"v_pk_fma_f32 v[48:49], v[32:33], v[212:213], v[48:49]\n"
"v_pk_fma_f32 v[50:51], v[34:35], v[220:221], v[50:51]\n"
"v_pk_fma_f32 v[52:53], v[36:37], v[228:229], v[52:53]\n"
"v_pk_fma_f32 v[54:55], v[38:39], v[176:177], v[54:55]\n"
"v_pk_fma_f32 v[48:49], v[8:9], v[210:211], v[48:49]\n"
"v_pk_fma_f32 v[50:51], v[10:11], v[218:219], v[50:51]\n"
"v_pk_fma_f32 v[52:53], v[12:13], v[226:227], v[52:53]\n"
"v_pk_fma_f32 v[54:55], v[14:15], v[234:235], v[54:55]\n"
"v_pk_fma_f32 v[48:49], v[16:17], v[208:209], v[48:49]\n"
"v_pk_fma_f32 v[50:51], v[18:19], v[216:217], v[50:51]\n"
"v_pk_fma_f32 v[52:53], v[20:21], v[224:225], v[52:53]\n"
"v_pk_fma_f32 v[54:55], v[22:23], v[232:233], v[54:55]\n"
"s_waitcnt vmcnt(6)\n"
"v_lshlrev_b32 v40, 16, v64\n"
"v_and_b32 v41, 0xffff0000, v64\n"
"v_lshlrev_b32 v42, 16, v65\n"
"v_and_b32 v43, 0xffff0000, v65\n"
"v_lshlrev_b32 v44, 16, v66\n"
"v_and_b32 v45, 0xffff0000, v66\n"
"v_lshlrev_b32 v46, 16, v67\n"
"v_and_b32 v47, 0xffff0000, v67\n"
"v_pk_mul_f32 v[48:49], v[48:49], v[40:41]\n"
"v_pk_mul_f32 v[50:51], v[50:51], v[42:43]\n"
"v_pk_mul_f32 v[52:53], v[52:53], v[44:45]\n"
"v_pk_mul_f32 v[54:55], v[54:55], v[46:47]\n"
"s_mov_b32 s63, 1064960\n"
"buffer_store_dwordx4 v[144:147], v252, s[20:23], s63 offen offset:0 sc1\n"
"s_mov_b32 s63, 1069056\n"
"buffer_store_dwordx4 v[148:151], v252, s[20:23], s63 offen offset:0 sc1\n"
"s_waitcnt vmcnt(7)\n"
"v_lshlrev_b32 v24, 16, v68\n"
"v_and_b32 v25, 0xffff0000, v68\n"
"v_lshlrev_b32 v26, 16, v69\n"
"v_and_b32 v27, 0xffff0000, v69\n"
"v_lshlrev_b32 v28, 16, v70\n"
"v_and_b32 v29, 0xffff0000, v70\n"
"v_lshlrev_b32 v30, 16, v71\n"
"v_and_b32 v31, 0xffff0000, v71\n"
"v_pk_mul_f32 v[56:57], v[32:33], v[214:215]\n"
"v_pk_mul_f32 v[58:59], v[34:35], v[222:223]\n"
"v_pk_mul_f32 v[60:61], v[36:37], v[230:231]\n"
"v_pk_mul_f32 v[62:63], v[38:39], v[178:179]\n"
"v_pk_fma_f32 v[56:57], v[8:9], v[212:213], v[56:57]\n"
"v_pk_fma_f32 v[58:59], v[10:11], v[220:221], v[58:59]\n"
"v_pk_fma_f32 v[60:61], v[12:13], v[228:229], v[60:61]\n"
"v_pk_fma_f32 v[62:63], v[14:15], v[176:177], v[62:63]\n"
"v_pk_fma_f32 v[56:57], v[16:17], v[210:211], v[56:57]\n"
"v_pk_fma_f32 v[58:59], v[18:19], v[218:219], v[58:59]\n"
"v_pk_fma_f32 v[60:61], v[20:21], v[226:227], v[60:61]\n"
"v_pk_fma_f32 v[62:63], v[22:23], v[234:235], v[62:63]\n"
"v_pk_fma_f32 v[56:57], v[24:25], v[208:209], v[56:57]\n"
"v_pk_fma_f32 v[58:59], v[26:27], v[216:217], v[58:59]\n"
"v_pk_fma_f32 v[60:61], v[28:29], v[224:225], v[60:61]\n"
"v_pk_fma_f32 v[62:63], v[30:31], v[232:233], v[62:63]\n"
"s_waitcnt vmcnt(6)\n"
"v_lshlrev_b32 v40, 16, v72\n"
"v_and_b32 v41, 0xffff0000, v72\n"
"v_lshlrev_b32 v42, 16, v73\n"
"v_and_b32 v43, 0xffff0000, v73\n"
"v_lshlrev_b32 v44, 16, v74\n"
"v_and_b32 v45, 0xffff0000, v74\n"
"v_lshlrev_b32 v46, 16, v75\n"
"v_and_b32 v47, 0xffff0000, v75\n"
"v_pk_mul_f32 v[56:57], v[56:57], v[40:41]\n"
"v_pk_mul_f32 v[58:59], v[58:59], v[42:43]\n"
"v_pk_mul_f32 v[60:61], v[60:61], v[44:45]\n"
"v_pk_mul_f32 v[62:63], v[62:63], v[46:47]\n"
"v_cvt_pk_bf16_f32 v160, v56, v48\n"
"v_cvt_pk_bf16_f32 v164, v57, v49\n"
"v_cvt_pk_bf16_f32 v168, v58, v50\n"
"v_cvt_pk_bf16_f32 v172, v59, v51\n"
"v_cvt_pk_bf16_f32 v180, v60, v52\n"
"v_cvt_pk_bf16_f32 v184, v61, v53\n"
"v_cvt_pk_bf16_f32 v188, v62, v54\n"
"v_cvt_pk_bf16_f32 v192, v63, v55\n"
"s_mov_b32 s63, 1073152\n"
"buffer_store_dwordx4 v[152:155], v252, s[20:23], s63 offen offset:0 sc1\n"
"s_mov_b32 s63, 1077248\n"
"buffer_store_dwordx4 v[156:159], v252, s[20:23], s63 offen offset:0 sc1\n"
"ds_write_b128 v206, v[160:163] offset:8192\n"
"ds_write_b128 v206, v[164:167] offset:8320\n"
"ds_write_b128 v206, v[168:171] offset:8448\n"
"ds_write_b128 v206, v[172:175] offset:8576\n"
"ds_write_b128 v206, v[180:183] offset:8704\n"
"ds_write_b128 v206, v[184:187] offset:8832\n"
"ds_write_b128 v206, v[188:191] offset:8960\n"
"ds_write_b128 v206, v[192:195] offset:9088\n"
"s_waitcnt lgkmcnt(0)\n"
"ds_read_b128 v[160:163], v207 offset:8192\n"
"ds_read_b128 v[164:167], v207 offset:8320\n"
"ds_read_b128 v[168:171], v207 offset:8448\n"
"ds_read_b128 v[172:175], v207 offset:8576\n"
"ds_read_b128 v[180:183], v207 offset:8704\n"
"ds_read_b128 v[184:187], v207 offset:8832\n"
"ds_read_b128 v[188:191], v207 offset:8960\n"
"ds_read_b128 v[192:195], v207 offset:9088\n"
"s_mov_b32 s63, 1048576\n"
"s_waitcnt lgkmcnt(7)\n"
"buffer_store_dwordx4 v[160:163], v252, s[20:23], s63 offen offset:128 sc1\n"
"s_mov_b32 s63, 1052672\n"
"s_waitcnt lgkmcnt(6)\n"
"buffer_store_dwordx4 v[164:167], v252, s[20:23], s63 offen offset:128 sc1\n"
"s_mov_b32 s63, 1056768\n"
"s_waitcnt lgkmcnt(5)\n"
"buffer_store_dwordx4 v[168:171], v252, s[20:23], s63 offen offset:128 sc1\n"
"s_mov_b32 s63, 1060864\n"
"s_waitcnt lgkmcnt(4)\n"
"buffer_store_dwordx4 v[172:175], v252, s[20:23], s63 offen offset:128 sc1\n"
"s_mov_b32 s63, 1064960\n"
"s_waitcnt lgkmcnt(3)\n"
"buffer_store_dwordx4 v[180:183], v252, s[20:23], s63 offen offset:128 sc1\n"
"s_mov_b32 s63, 1069056\n"
"s_waitcnt lgkmcnt(2)\n"
"buffer_store_dwordx4 v[184:187], v252, s[20:23], s63 offen offset:128 sc1\n"
"s_mov_b32 s63, 1073152\n"
"s_waitcnt lgkmcnt(1)\n"
"buffer_store_dwordx4 v[188:191], v252, s[20:23], s63 offen offset:128 sc1\n"
"s_mov_b32 s63, 1077248\n"
"s_waitcnt lgkmcnt(0)\n"
"buffer_store_dwordx4 v[192:195], v252, s[20:23], s63 offen offset:128 sc1\n"
"s_lshl_b32 s63, s77, 4\n"
"s_add_u32 s63, s63, s76\n"
"s_lshl_b32 s63, s63, 3\n"
"s_add_u32 s63, s63, 32768\n"
"s_waitcnt vmcnt(0)\n"
"buffer_store_dwordx2 v[2:3], v4, s[20:23], s63 offen sc1\n"
"s_cmp_lg_u32 s62, 0\n"
"s_cbranch_scc1 Lnoep_%=\n"
"s_add_u32 s63, s75, 4096\n"
"s_mov_b32 s99, 0\n"
"Lep_%=:\n"
"buffer_load_dwordx4 v[64:67], v1, s[20:23], s63 offen offset:0 sc1\n"
"buffer_load_dwordx4 v[68:71], v1, s[20:23], s63 offen offset:1024 sc1\n"
"buffer_load_dwordx4 v[72:75], v1, s[20:23], s63 offen offset:2048 sc1\n"
"buffer_load_dwordx4 v[76:79], v1, s[20:23], s63 offen offset:3072 sc1\n"
"s_waitcnt vmcnt(0) lgkmcnt(0)\n"
"v_xor_b32 v22, v65, v2\n"
"v_bitop3_b32 v22, v22, v67, v2 bitop3:0xf6\n"
"v_bitop3_b32 v22, v22, v69, v2 bitop3:0xf6\n"
"v_bitop3_b32 v22, v22, v71, v2 bitop3:0xf6\n"
"v_bitop3_b32 v22, v22, v73, v2 bitop3:0xf6\n"
"v_bitop3_b32 v22, v22, v75, v2 bitop3:0xf6\n"
"v_bitop3_b32 v22, v22, v77, v2 bitop3:0xf6\n"
"v_bitop3_b32 v22, v22, v79, v2 bitop3:0xf6\n"
"v_cmp_ne_u32 vcc, 0, v22\n"
"s_cbranch_vccz Lnoep_%=\n"
"s_sleep 2\n"
"s_add_u32 s99, s99, 1\n"
"s_cmp_lt_u32 s99, 0x8000\n"
"s_cbranch_scc1 Lep_%=\n"
"Lnoep_%=:\n"
"s_barrier\n"
"s_or_b32 s63, s62, s61\n"
"s_cbranch_scc1 Lnoep2_%=\n"
"buffer_store_dwordx2 v[2:3], v4, s[20:23], 0 offen sc1\n"
"Lnoep2_%=:\n"
"v_lshrrev_b32 v40, 4, v0\n"
"v_and_b32 v41, 15, v0\n"
"v_bfe_u32 v42, v0, 2, 2\n"
"v_and_b32 v43, 3, v0\n"
"v_and_b32 v44, 31, v0\n"
"v_lshrrev_b32 v45, 5, v0\n"
"v_and_b32 v46, 7, v0\n"
"v_lshrrev_b32 v47, 3, v0\n"
"s_lshl_b32 s64, s61, 5\n"
"v_lshl_add_u32 v49, v43, 3, s64\n"
"v_and_b32 v50, 24, v0\n"
"v_lshlrev_b32 v50, 2, v50\n"
"v_xor_b32 v49, v49, v50\n"
"v_lshlrev_b32 v51, 7, v42\n"
"v_lshl_add_u32 v51, v40, 10, v51\n"
"v_add_u32 v5, v49, v51\n"
"v_add_u32 v249, 114688, v5\n"
"v_lshlrev_b32 v49, 4, v43\n"
"v_xor_b32 v49, v49, v50\n"
"s_add_u32 s64, s75, 98304\n"
"v_add3_u32 v232, s64, v49, v51\n"
"v_xor_b32 v233, 64, v232\n"
"s_and_b32 s76, s80, 1\n"
"s_lshr_b32 s77, s80, 1\n"
"s_lshr_b32 s78, s81, 3\n"
"s_and_b32 s79, s81, 7\n"
"s_and_b32 s80, s61, 1\n"
"s_lshr_b32 s81, s61, 1\n"
"s_mul_i32 s64, s76, 917504\n"
"s_lshl_b32 s65, s61, 14\n"
"s_add_u32 s64, s64, s65\n"
"s_lshl_b32 s65, s79, 7\n"
"s_add_u32 s64, s64, s65\n"
"v_and_b32 v50, 2, v47\n"
"v_xor_b32 v49, v46, v50\n"
"v_lshlrev_b32 v49, 4, v49\n"
"v_lshl_add_u32 v49, v47, 10, v49\n"
"v_add_u32 v18, s64, v49\n"
"v_xor_b32 v49, 64, v18\n"
"v_add_u32 v19, 7168, v49\n"
"v_add_u32 v20, 63488, v18\n"
"v_add_u32 v21, 63488, v19\n"
"s_add_u32 s6, s77, s78\n"
"s_lshl_b32 s88, s6, 11\n"
"s_add_u32 s89, s88, 2048\n"
"s_cmp_eq_u32 s89, 14336\n"
"s_cselect_b32 s89, 0, s89\n"
"s_add_u32 s90, s89, 2048\n"
"s_cmp_eq_u32 s90, 14336\n"
"s_cselect_b32 s90, 0, s90\n"
"s_add_u32 s91, s90, 2048\n"
"s_cmp_eq_u32 s91, 14336\n"
"s_cselect_b32 s91, 0, s91\n"
"s_add_u32 s92, s91, 2048\n"
"s_cmp_eq_u32 s92, 14336\n"
"s_cselect_b32 s92, 0, s92\n"
"s_add_u32 s93, s92, 2048\n"
"s_cmp_eq_u32 s93, 14336\n"
"s_cselect_b32 s93, 0, s93\n"
"s_add_u32 s94, s93, 2048\n"
"s_cmp_eq_u32 s94, 14336\n"
"s_cselect_b32 s94, 0, s94\n"
"s_lshl_b32 s77, s77, 2\n"
"s_add_u32 s77, s77, s78\n"
"s_lshl_b32 s78, s77, 1\n"
"s_add_u32 s78, s78, s76\n"
"s_mul_i32 s65, s77, 458752\n"
"s_lshl_b32 s6, s76, 2\n"
"s_add_u32 s6, s6, s61\n"
"s_mul_i32 s64, s6, 14336\n"
"s_add_u32 s65, s65, s64\n"
"s_add_u32 s65, s65, 3375104\n"
"s_mul_i32 s64, s76, 229376\n"
"s_sub_u32 s65, s65, s64\n"
"s_lshl_b32 s83, s64, 1\n"
"s_sub_u32 s83, s83, 229376\n"
"v_lshlrev_b32 v49, 4, v41\n"
"v_lshl_add_u32 v49, v40, 9, v49\n"
"v_add_u32 v49, s65, v49\n"
"s_mul_i32 s64, s81, 114688\n"
"s_lshl_b32 s65, s80, 8\n"
"v_add_u32 v49, s65, v49\n"
"v_add_u32 v58, s64, v49\n"
"s_sub_u32 s64, 114688, s64\n"
"v_add_u32 v60, s64, v49\n"
"v_xor_b32 v59, 256, v58\n"
"v_xor_b32 v61, 256, v60\n"
"s_lshr_b32 s64, s77, 1\n"
"s_lshl_b32 s64, s64, 11\n"
"s_lshl_b32 s6, s6, 8\n"
"s_add_u32 s64, s64, s6\n"
"s_add_u32 s64, s64, 524288\n"
"v_min_u32 v49, 13, v0\n"
"v_lshl_add_u32 v63, v49, 3, s64\n"
"s_lshr_b32 s64, s62, 5\n"
"s_lshl_b32 s64, s64, 7\n"
"s_add_u32 s64, s64, 32768\n"
"v_min_u32 v49, 15, v0\n"
"v_lshl_add_u32 v62, v49, 3, s64\n"
"s_lshl_b32 s77, s62, 1\n"
"s_add_u32 s77, s77, s81\n"
"s_lshl_b32 s5, s77, 12\n"
"s_lshl_b32 s64, s80, 11\n"
"s_add_u32 s64, s64, s5\n"
"s_add_u32 s4, s64, 1048576\n"
"s_add_u32 s97, s64, 10485760\n"
"s_lshl_b32 s98, s80, 9\n"
"s_add_u32 s98, s98, s77\n"
"s_lshl_b32 s98, s98, 5\n"
"s_add_u32 s98, s98, 786432\n"
"s_lshl_b32 s64, s80, 3\n"
"s_add_u32 s96, s5, s64\n"
"s_add_u32 s96, s96, 4\n"
"s_mul_i32 s6, s81, 25792\n"
"s_mul_i32 s64, s80, 8320\n"
"s_add_u32 s95, s6, s64\n"
"s_lshl_b32 s64, s62, 2\n"
"s_add_u32 s64, s64, s61\n"
"s_mul_i32 s2, s64, 3072\n"
"s_xor_b32 s64, s64, 4\n"
"s_mul_i32 s3, s64, 3072\n"
"s_add_u32 s2, s2, 12582912\n"
"s_add_u32 s3, s3, 12582912\n"
"s_lshl_b32 s65, s80, 8\n"
"s_sub_u32 s82, s6, s65\n"
"v_add_u32 v50, v40, v42\n"
"v_lshlrev_b32 v50, 4, v50\n"
"v_mul_u32_u24 v51, 4160, v43\n"
"s_add_u32 s64, s82, 256\n"
"v_add3_u32 v222, s64, v50, v51\n"
"v_lshrrev_b32 v50, 1, v41\n"
"v_lshlrev_b32 v50, 2, v50\n"
"v_sub_u32 v50, v40, v50\n"
"v_lshlrev_b32 v50, 4, v50\n"
"v_and_b32 v51, 1, v0\n"
"v_mul_u32_u24 v51, 4576, v51\n"
"s_add_u32 s64, s82, 17344\n"
"v_add3_u32 v223, s64, v50, v51\n"
"s_add_u32 s6, s6, 16640\n"
"v_min_u32 v50, 27, v44\n"
"v_lshl_add_u32 v50, v50, 4, s6\n"
"v_mul_u32_u24 v51, 4576, v45\n"
"v_add_u32 v243, v50, v51\n"
"s_lshl_b32 s64, s80, 11\n"
"s_add_u32 s6, s6, s64\n"
"v_add_u32 v242, s6, v1\n"
"v_sub_u32 v50, 63, v0\n"
"v_bfe_u32 v51, v50, 1, 3\n"
"v_and_b32 v52, 48, v50\n"
"v_and_b32 v50, 1, v50\n"
"v_lshl_add_u32 v51, v50, 3, v51\n"
"v_add_lshl_u32 v51, v51, v52, 4\n"
"s_xor_b32 s64, s4, 2048\n"
"v_add_u32 v234, s64, v51\n"
"v_cmp_eq_u32 vcc, 15, v41\n"
"v_mov_b32 v52, 112\n"
"v_mov_b32 v51, 0xffffff80\n"
"v_cndmask_b32 v52, v52, -16, vcc\n"
"v_cmp_eq_u32 vcc, 1, v50\n"
"s_lshl_b32 s64, s80, 31\n"
"s_nop 0\n"
"v_cndmask_b32 v52, v52, v51, vcc\n"
"v_add_u32 v240, v52, v234\n"
"v_cmp_eq_u32 vcc, 63, v0\n"
"v_or_b32 v50, s64, v240\n"
"s_nop 0\n"
"v_cndmask_b32 v241, v240, v50, vcc\n"
"v_and_b32 v50, 1, v40\n"
"v_lshl_add_u32 v50, v45, 3, v50\n"
"v_xor_b32 v50, v50, v0\n"
"s_lshl_b32 s64, s61, 13\n"
"s_add_u32 s64, s64, 65536\n"
"v_lshl_add_u32 v244, v50, 4, s64\n"
"v_lshrrev_b32 v50, 2, v44\n"
"v_lshlrev_b32 v50, 1, v50\n"
"v_and_b32 v51, 1, v0\n"
"v_xor_b32 v50, v50, v51\n"
"v_and_b32 v51, 2, v0\n"
"v_lshlrev_b32 v51, 2, v51\n"
"v_xor_b32 v50, v50, v51\n"
"v_lshl_add_u32 v50, v43, 4, v50\n"
"v_lshl_add_u32 v50, v45, 6, v50\n"
"s_and_b32 s64, s61, 2\n"
"s_add_u32 s64, s64, s61\n"
"s_lshl_b32 s64, s64, 12\n"
"s_add_u32 s64, s64, 65536\n"
"v_lshl_add_u32 v245, v50, 4, s64\n"
"v_xor_b32 v246, 16, v245\n"
"s_xor_b32 s64, s61, 1\n"
"s_mul_i32 s64, s64, 12288\n"
"v_add_u32 v253, s64, v1\n"
"s_xor_b32 s64, s61, 2\n"
"s_mul_i32 s64, s64, 12288\n"
"v_add_u32 v254, s64, v1\n"
"s_xor_b32 s64, s61, 3\n"
"s_mul_i32 s64, s64, 12288\n"
"v_add_u32 v255, s64, v1\n"
"s_mul_i32 s64, s61, 12288\n"
"v_add_u32 v247, s64, v1\n"
"s_lshl_b32 s64, s61, 11\n"
"s_add_u32 s64, s64, 114688\n"
"v_and_b32 v50, 16, v0\n"
"v_lshlrev_b32 v50, 1, v50\n"
"v_xor_b32 v50, v50, v1\n"
"v_add_u32 v248, s64, v50\n"
"s_lshl_b32 s64, s78, 16\n"
"s_lshl_b32 s65, s61, 14\n"
"s_add_u32 s64, s64, s65\n"
"s_lshl_b32 s65, s79, 7\n"
"s_add_u32 s64, s64, s65\n"
"v_lshlrev_b32 v50, 4, v40\n"
"v_lshl_add_u32 v50, v41, 10, v50\n"
"v_add_u32 v250, s64, v50\n"
"s_lshl_b32 s6, s79, 2\n"
"s_add_u32 s6, s6, s61\n"
"s_lshl_b32 s64, s6, 16\n"
"s_lshl_b32 s65, s78, 7\n"
"s_add_u32 s64, s64, s65\n"
"s_add_u32 s64, s64, 10485760\n"
"v_lshlrev_b32 v50, 4, v46\n"
"v_lshl_add_u32 v50, v47, 12, v50\n"
"v_add_u32 v251, s64, v50\n"
"s_lshl_b32 s64, s6, 9\n"
"s_lshr_b32 s65, s78, 4\n"
"s_lshl_b32 s65, s65, 14\n"
"s_add_u32 s64, s64, s65\n"
"s_add_u32 s64, s64, 786432\n"
"v_min_u32 v49, 15, v0\n"
"v_lshl_add_u32 v252, v49, 5, s64\n"
"s_lshl_b32 s64, s78, 9\n"
"s_lshl_b32 s65, s80, 8\n"
"s_add_u32 s64, s64, s65\n"
"s_lshl_b32 s65, s81, 3\n"
"s_add_u32 s64, s64, s65\n"
"s_add_u32 s64, s64, 4096\n"
"v_lshl_add_u32 v235, v41, 4, s64\n"
"s_mov_b32 s82, 32768\n"
"s_lshl_b32 s64, s81, 4\n"
"s_lshr_b32 s60, s60, s64\n"
"s_and_b32 s60, s60, 0xffff\n"
"s_lshl_b32 s64, s80, 8\n"
"s_sub_u32 s64, 196, s64\n"
"v_and_b32 v51, 48, v0\n"
"v_lshl_add_u32 v50, v41, 2, v51\n"
"v_add_u32 v50, s64, v50\n"
"v_mov_b32 v51, s60\n"
"v_add_u32 v228, 0, v50\n"
"v_cmp_eq_u32 vcc, 0, v228\n"
"v_min_u32 v228, 0x7fffffff, v228\n"
"s_nop 0\n"
"v_cndmask_b32 v224, 0, v51, vcc\n"
"v_add_u32 v229, 4, v50\n"
"v_cmp_eq_u32 vcc, 0, v229\n"
"v_min_u32 v229, 0x7fffffff, v229\n"
"s_nop 0\n"
"v_cndmask_b32 v225, 0, v51, vcc\n"
"v_add_u32 v230, 8, v50\n"
"v_cmp_eq_u32 vcc, 0, v230\n"
"v_min_u32 v230, 0x7fffffff, v230\n"
"s_nop 0\n"
"v_cndmask_b32 v226, 0, v51, vcc\n"
"v_add_u32 v231, 12, v50\n"
"v_cmp_eq_u32 vcc, 0, v231\n"
"v_min_u32 v231, 0x7fffffff, v231\n"
"s_nop 0\n"
"v_cndmask_b32 v227, 0, v51, vcc\n"
"s_add_u32 m0, s75, 131072\n"
"s_lshl_b32 s63, s88, 6\n"
"buffer_load_dwordx4 v18, s[24:27], s63 offen offset:0 lds\n"
"buffer_load_dwordx4 v19, s[24:27], s63 offen offset:1024 lds\n"
"buffer_load_dwordx4 v20, s[24:27], s63 offen offset:2048 lds\n"
"buffer_load_dwordx4 v21, s[24:27], s63 offen offset:3072 lds\n"
"s_add_u32 m0, s75, 147456\n"
"s_lshl_b32 s63, s89, 6\n"
"buffer_load_dwordx4 v18, s[24:27], s63 offen offset:0 lds\n"
"buffer_load_dwordx4 v19, s[24:27], s63 offen offset:1024 lds\n"
"buffer_load_dwordx4 v20, s[24:27], s63 offen offset:2048 lds\n"
"buffer_load_dwordx4 v21, s[24:27], s63 offen offset:3072 lds\n"
"s_add_u32 m0, s95, 0\n"
"s_add_u32 s63, s96, 0\n"
"buffer_load_dwordx4 v1, s[28:31], s63 offen offset:0 lds\n"
"s_waitcnt vmcnt(5)\n"
"ds_read_b64_tr_b16 a[128:129], v232 offset:32768\n"
"ds_read_b64_tr_b16 a[130:131], v232 offset:33280\n"
"ds_read_b64_tr_b16 a[132:133], v232 offset:32776\n"
"buffer_load_dwordx4 v1, s[28:31], s63 offen offset:1024 lds\n"
"ds_read_b64_tr_b16 a[134:135], v232 offset:33288\n"
"ds_read_b64_tr_b16 a[136:137], v233 offset:32768\n"
"buffer_load_dwordx4 v1, s[28:31], s63 offen offset:2048 lds\n"
"buffer_load_dwordx4 v1, s[28:31], s63 offen offset:3072 lds\n"
"ds_read_b64_tr_b16 a[138:139], v233 offset:33280\n"
"ds_read_b64_tr_b16 a[140:141], v233 offset:32776\n"
"ds_read_b64_tr_b16 a[142:143], v233 offset:33288\n"
"s_waitcnt vmcnt(4)\n"
"ds_read_b64_tr_b16 a[144:145], v232 offset:49152\n"
"ds_read_b64_tr_b16 a[146:147], v232 offset:49664\n"
"ds_read_b64_tr_b16 a[148:149], v232 offset:49160\n"
"ds_read_b64_tr_b16 a[150:151], v232 offset:49672\n"
"ds_read_b64_tr_b16 a[152:153], v233 offset:49152\n"
"ds_read_b64_tr_b16 a[154:155], v233 offset:49664\n"
"ds_read_b64_tr_b16 a[156:157], v233 offset:49160\n"
"ds_read_b64_tr_b16 a[158:159], v233 offset:49672\n"
"s_add_u32 m0, s95, 4160\n"
"s_add_u32 s63, s96, 4\n"
"buffer_load_dwordx4 v1, s[28:31], s63 offen offset:0 lds\n"
"buffer_load_dwordx4 v1, s[28:31], s63 offen offset:1024 lds\n"
"buffer_load_dwordx4 v1, s[28:31], s63 offen offset:2048 lds\n"
"buffer_load_dwordx4 v1, s[28:31], s63 offen offset:3072 lds\n"
"buffer_load_dword v10, v228, s[28:31], s5 offen\n"
"buffer_load_dword v11, v229, s[28:31], s5 offen\n"
"buffer_load_dword v12, v230, s[28:31], s5 offen\n"
"buffer_load_dword v13, v231, s[28:31], s5 offen\n"
"buffer_load_dwordx2 v[38:39], v62, s[20:23], 0 offen offset:0 sc1\n"
"s_waitcnt vmcnt(0)\n"
"v_cmp_ne_u64 vcc, v[38:39], v[2:3]\n"
"s_cbranch_vccnz LslowK_h_%=\n"
"LokK_h_%=:\n"
"buffer_load_dwordx4 v[192:195], v234, s[20:23], 0 offen offset:1024 sc1\n"
"buffer_load_dwordx4 v[196:199], v234, s[20:23], 0 offen sc1\n"
"buffer_load_dword v240, v240, s[20:23], 0 offen offset:1024 sc1\n"
"buffer_load_dword v241, v241, s[20:23], 0 offen sc1\n"
"s_add_u32 m0, s75, 131072\n"
"s_lshl_b32 s63, s90, 6\n"
"s_waitcnt lgkmcnt(0)\n"
"buffer_load_dwordx4 v18, s[24:27], s63 offen offset:0 lds\n"
"buffer_load_dwordx4 v19, s[24:27], s63 offen offset:1024 lds\n"
"v_mov_b32 v200, 0\n"
"buffer_load_dwordx4 v20, s[24:27], s63 offen offset:2048 lds\n"
"v_mov_b32 v201, 0\n"
"v_mov_b32 v202, 0\n"
"v_mov_b32 v203, 0\n"
"buffer_load_dwordx4 v21, s[24:27], s63 offen offset:3072 lds\n"
"ds_write_b128 v243, v[200:203] offset:4096\n"
"s_add_u32 m0, s75, 147456\n"
"s_lshl_b32 s63, s91, 6\n"
"buffer_load_dwordx4 v18, s[24:27], s63 offen offset:0 lds\n"
"buffer_load_dwordx2 v[38:39], v63, s[20:23], 0 offen offset:0 sc1\n"
"buffer_load_dwordx4 v19, s[24:27], s63 offen offset:1024 lds\n"
"buffer_load_dwordx4 v20, s[24:27], s63 offen offset:2048 lds\n"
"s_waitcnt vmcnt(8)\n"
"ds_write_b128 v242, v[192:195] offset:4576\n"
"v_alignbit_b32 v204, v193, v192, 16\n"
"v_alignbit_b32 v205, v194, v193, 16\n"
"v_alignbit_b32 v206, v195, v194, 16\n"
"buffer_load_dwordx4 v21, s[24:27], s63 offen offset:3072 lds\n"
"v_alignbit_b32 v207, v240, v195, 16\n"
"ds_write_b128 v242, v[204:207] offset:0\n"
"s_add_u32 m0, s75, 98304\n"
"s_lshl_b32 s63, s92, 6\n"
"buffer_load_dwordx4 v18, s[24:27], s63 offen offset:0 lds\n"
"ds_write_b128 v242, v[196:199] offset:5600\n"
"v_alignbit_b32 v208, v197, v196, 16\n"
"v_alignbit_b32 v209, v198, v197, 16\n"
"v_alignbit_b32 v210, v199, v198, 16\n"
"v_alignbit_b32 v211, v241, v199, 16\n"
"buffer_load_dwordx4 v19, s[24:27], s63 offen offset:1024 lds\n"
"buffer_load_dwordx4 v20, s[24:27], s63 offen offset:2048 lds\n"
"ds_write_b128 v242, v[208:211] offset:1024\n"
"buffer_load_dwordx4 v21, s[24:27], s63 offen offset:3072 lds\n"
"s_waitcnt vmcnt(9)\n"
"ds_read_b64_tr_b16 a[160:161], v232 offset:32768\n"
"s_add_u32 m0, s75, 114688\n"
"s_waitcnt vmcnt(7)\n"
"v_cmp_ne_u64 vcc, v[38:39], v[2:3]\n"
"s_cbranch_vccnz LslowA_h_%=\n"
"LokA_h_%=:\n"
"s_lshl_b32 s63, s93, 6\n"
"buffer_load_dwordx4 v18, s[24:27], s63 offen offset:0 lds\n"
"ds_read_b64_tr_b16 a[162:163], v232 offset:33280\n"
"buffer_load_dwordx4 v19, s[24:27], s63 offen offset:1024 lds\n"
"s_waitcnt lgkmcnt(2)\n"
"s_barrier\n"
"ds_read_b64_tr_b16 a[164:165], v232 offset:32776\n"
"ds_read_b128 a[0:3], v223 offset:3776\n"
"ds_read_b64_tr_b16 a[166:167], v232 offset:33288\n"
"ds_read_b64_tr_b16 a[168:169], v233 offset:32768\n"
"ds_read_b128 a[4:7], v222 offset:128\n"
"ds_read_b128 a[8:11], v222 offset:640\n"
"ds_read_b128 a[12:15], v223 offset:3264\n"
"buffer_load_dwordx4 v20, s[24:27], s63 offen offset:2048 lds\n"
"ds_read_b64_tr_b16 a[170:171], v233 offset:33280\n"
"ds_read_b64_tr_b16 a[172:173], v233 offset:32776\n"
"ds_read_b128 a[16:19], v222 offset:1152\n"
"ds_read_b64_tr_b16 a[174:175], v233 offset:33288\n"
"ds_read_b128 a[20:23], v223 offset:2752\n"
"s_mov_b32 s42, 0\n"
"s_waitcnt vmcnt(0) lgkmcnt(0)\n"
"s_nop 7\n"
"s_branch Ljoin_%=\n"
"LslowK_h_%=:\n"
"s_mov_b32 s99, 0\n"
"LslK_h_%=:\n"
"buffer_load_dwordx2 v[38:39], v62, s[20:23], 0 offen offset:0 sc1\n"
"s_waitcnt vmcnt(0)\n"
"v_cmp_ne_u64 vcc, v[38:39], v[2:3]\n"
"s_cbranch_vccz LokK_h_%=\n"
"s_sleep 1\n"
"s_add_u32 s99, s99, 1\n"
"s_cmp_lt_u32 s99, 0x8000\n"
"s_cbranch_scc1 LslK_h_%=\n"
"s_branch LokK_h_%=\n"
"LslowA_h_%=:\n"
"s_mov_b32 s99, 0\n"
"LslA_h_%=:\n"
"buffer_load_dwordx2 v[38:39], v63, s[20:23], 0 offen offset:0 sc1\n"
"s_waitcnt vmcnt(0)\n"
"v_cmp_ne_u64 vcc, v[38:39], v[2:3]\n"
"s_cbranch_vccz LokA_h_%=\n"
"s_sleep 1\n"
"s_add_u32 s99, s99, 1\n"
"s_cmp_lt_u32 s99, 0x8000\n"
"s_cbranch_scc1 LslA_h_%=\n"
"s_branch LokA_h_%=\n"
"Lend_%=:\n"
      : "+v"(tv)
      : "s"(__builtin_amdgcn_kernarg_segment_ptr()), "s"(b), "s"(tstart), "s"((unsigned)(size_t)smem)
      : "v8","v9","v10","v11","v12","v13","v14","v15","v16","v17","v18","v19","v20","v21","v22","v23","v24","v25","v26","v27","v28","v29","v30","v31",
"v32","v33","v34","v35","v36","v37","v38","v39","v40","v41","v42","v43","v44","v45","v46","v47","v48","v49","v50","v51","v52","v53","v54","v55",
"v56","v57","v58","v59","v60","v61","v62","v63","v64","v65","v66","v67","v68","v69","v70","v71","v72","v73","v74","v75","v76","v77","v78","v79",
"v80","v81","v82","v83","v84","v85","v86","v87","v88","v89","v90","v91","v92","v93","v94","v95","v96","v97","v98","v99","v100","v101","v102","v103",
"v104","v105","v106","v107","v108","v109","v110","v111","v112","v113","v114","v115","v116","v117","v118","v119","v120","v121","v122","v123","v124","v125","v126","v127",
"v128","v129","v130","v131","v132","v133","v134","v135","v136","v137","v138","v139","v140","v141","v142","v143","v144","v145","v146","v147","v148","v149","v150","v151",
"v152","v153","v154","v155","v156","v157","v158","v159","v160","v161","v162","v163","v164","v165","v166","v167","v168","v169","v170","v171","v172","v173","v174","v175",
"v176","v177","v178","v179","v180","v181","v182","v183","v184","v185","v186","v187","v188","v189","v190","v191","v192","v193","v194","v195","v196","v197","v198","v199",
"v200","v201","v202","v203","v204","v205","v206","v207","v208","v209","v210","v211","v212","v213","v214","v215","v216","v217","v218","v219","v220","v221","v222","v223",
"v224","v225","v226","v227","v228","v229","v230","v231","v232","v233","v234","v235","v236","v237","v238","v239","v240","v241","v242","v243","v244","v245","v246","v247",
"v248","v249","v250","v251","v252","v253","v254","v255","a0","a1","a2","a3","a4","a5","a6","a7","a8","a9","a10","a11","a12","a13","a14","a15",
"a16","a17","a18","a19","a20","a21","a22","a23","a24","a25","a26","a27","a28","a29","a30","a31","a32","a33","a34","a35","a36","a37","a38","a39",
"a40","a41","a42","a43","a44","a45","a46","a47","a48","a49","a50","a51","a52","a53","a54","a55","a56","a57","a58","a59","a60","a61","a62","a63",
"a64","a65","a66","a67","a68","a69","a70","a71","a72","a73","a74","a75","a76","a77","a78","a79","a80","a81","a82","a83","a84","a85","a86","a87",
"a88","a89","a90","a91","a92","a93","a94","a95","a96","a97","a98","a99","a100","a101","a102","a103","a104","a105","a106","a107","a108","a109","a110","a111",
"a112","a113","a114","a115","a116","a117","a118","a119","a120","a121","a122","a123","a124","a125","a126","a127","a128","a129","a130","a131","a132","a133","a134","a135",
"a136","a137","a138","a139","a140","a141","a142","a143","a144","a145","a146","a147","a148","a149","a150","a151","a152","a153","a154","a155","a156","a157","a158","a159",
"a160","a161","a162","a163","a164","a165","a166","a167","a168","a169","a170","a171","a172","a173","a174","a175","a176","a177","a178","a179","a180","a181","a182","a183",
"a184","a185","a186","a187","a188","a189","a190","a191","a192","a193","a194","a195","a196","a197","a198","a199","a200","a201","a202","a203","a204","a205","a206","a207",
"a208","a209","a210","a211","a212","a213","a214","a215","a216","a217","a218","a219","a220","a221","a222","a223","a224","a225","a226","a227","a228","a229","a230","a231",
"a232","a233","a234","a235","a236","a237","a238","a239","a240","a241","a242","a243","a244","a245","a246","a247","a248","a249","a250","a251","a252","a253","a254","a255",
"s6","s7","s8","s9","s10","s11","s12","s13","s14","s15","s16","s17","s18","s19","s20","s21","s22","s23","s24","s25","s26","s27","s28","s29",
"s30","s31","s34","s35","s36","s37","s38","s39","s40","s41","s42","s43","s44","s45","s46","s47","s48","s49","s50","s51","s52","s53","s54","s55",
"s56","s57","s58","s59","s60","s61","s62","s63","s64","s65","s66","s67","s68","s69","s70","s71","s72","s73","s74","s75","s76","s77","s78","s79",
"s80","s81","s82","s83","s84","s85","s86","s87","s88","s89","s90","s91","s92","s93","s94","s95","s96","s97","s98","s99","vcc","scc","memory");
}

extern "C" void kernel_launch(void* const* d_in, const int* in_sizes, int n_in, void* d_out, int out_size, void* d_ws,
                              size_t ws_size, hipStream_t stream) {
  const unsigned F = 0x00020000u, SD = 2048u * 512u * 2u, DF = 512u * 1792u * 2u, WSB = 15728640u;
  Args a = {{d_in[0], SD, F}, {d_in[7], DF, F}, {d_in[8], DF, F}, {d_ws, WSB, F}, {d_in[9], DF, F}, {d_in[4], SD, F},
            {d_out, SD, F},   {d_ws, WSB, F},   {d_in[1], SD, F}, {d_in[2], SD, F}, {d_in[3], 4096u, F}, d_in[5]};
  fused_h3_reglu<<<256, 256, 0, stream>>>(a);
}
```
